```python
import torch

def _softmax(x, axis=-1):
    return torch.softmax(x, dim=axis)
def _gelu(x):
    return torch.nn.functional.gelu(x, approximate="tanh")
def _silu(x):
    return torch.nn.functional.silu(x)
def _sigmoid(x):
    return torch.sigmoid(x)
def _layernorm(x, axis=-1, eps=1e-5):
    m = x.mean(dim=axis, keepdim=True)
    v = x.var(dim=axis, keepdim=True, unbiased=False)
    return (x - m) / torch.sqrt(v + eps)
def _scatter_add(tgt, idx, src, axis=0):
    return tgt.clone().index_add_(axis, idx, src)
def _segment_sum(src, seg, n):
    out = torch.zeros((n,) + src.shape[1:], dtype=src.dtype)
    return out.index_add_(0, seg, src)
def _pad(x, pw):
    flat = tuple(v for pair in reversed(pw) for v in pair)
    return torch.nn.functional.pad(x, flat)

kernel_name = 'sequential_preLN__gqa_block_cached__geglu_ffn__decode'

def setup_inputs():

    _w = (lambda a: (lambda t: t.to(torch.bfloat16) if t.is_floating_point() else t)(torch.as_tensor(a)))
    def _A_setup():
        B, Sq, D = 8, 1, 576
        Hq, Hkv, Sk, Dh = 32, 16, 4096, 128
        assert Hq % Hkv == 0, (Hq, Hkv)
        return {
            "x": _w((lambda *s: torch.randn(*s, dtype=torch.bfloat16) * 0.05)(B, Sq, D)),
            "k_cache": _w((lambda *s: torch.randn(*s, dtype=torch.bfloat16) * 3.2)(B, Hkv, Sk, Dh)),
            "v_cache": _w((lambda *s: torch.randn(*s, dtype=torch.bfloat16) * 0.4)(B, Hkv, Sk, Dh)),
            "wq": _w((lambda *s: torch.randn(*s, dtype=torch.bfloat16) * 3.2)(D, Hq * Dh) * (D ** -0.5)),
            "wo": _w((lambda *s: torch.randn(*s, dtype=torch.bfloat16) * 0.4)(Hq * Dh, D) * ((Hq * Dh) ** -0.5)),
        }
    _A_in = _A_setup()

    def _B_setup():
        B, S, D, F = 8, 1, 576, 4608
        return {
            "x": _w((lambda *s: torch.randn(*s, dtype=torch.bfloat16) * 0.05)(B, S, D)),
            "w_gate": _w((lambda *s: torch.randn(*s, dtype=torch.bfloat16) * 0.05)(D, F)),
            "w_up": _w((lambda *s: torch.randn(*s, dtype=torch.bfloat16) * 0.05)(D, F)),
            "w_down": _w((lambda *s: torch.randn(*s, dtype=torch.bfloat16) * 0.05)(F, D)),
        }
    _B_in = _B_setup()

    return {
        "A_x": _A_in["x"],
        "A_k_cache": _A_in["k_cache"],
        "A_v_cache": _A_in["v_cache"],
        "A_wq": _A_in["wq"],
        "A_wo": _A_in["wo"],
        "B_x": _B_in["x"],
        "B_w_gate": _B_in["w_gate"],
        "B_w_up": _B_in["w_up"],
        "B_w_down": _B_in["w_down"],
    }

def reference(A_x, A_k_cache, A_v_cache, A_wq, A_wo, B_x, B_w_gate, B_w_up, B_w_down):
    def _A_ref(x, k_cache, v_cache, wq, wo):
        B, Sq, D = x.shape
        Hq, Dh = 32, 128
        Hkv = k_cache.shape[1]
        rep = Hq // Hkv
        q = (lambda x, *d: x.permute(*d))((x @ wq).reshape(B, Sq, Hq, Dh), 0, 2, 1, 3)
        k = (lambda x, n, axis: torch.repeat_interleave(x, n, dim=axis))(k_cache, rep, axis=1)
        v = (lambda x, n, axis: torch.repeat_interleave(x, n, dim=axis))(v_cache, rep, axis=1)
        scores = torch.einsum("bhqd,bhkd->bhqk", q, k) * (Dh ** -0.5)
        p = _softmax(scores, axis=-1)
        o = torch.einsum("bhqk,bhkd->bhqd", p, v)
        return (lambda x, *d: x.permute(*d))(o, 0, 2, 1, 3).reshape(B, Sq, Hq * Dh) @ wo

    def _B_ref(x, w_gate, w_up, w_down):
        return (_gelu(x @ w_gate) * (x @ w_up)) @ w_down

    x = A_x
    h = x + _A_ref(_layernorm(x, axis=-1), A_k_cache, A_v_cache, A_wq, A_wo)
    h2 = h + _B_ref(_layernorm(h, axis=-1), B_w_gate, B_w_up, B_w_down)
    return h2
```

```cpp
#include <hip/hip_runtime.h>
#include <hip/hip_bf16.h>
#include <stdint.h>

#define MIMIC 0

typedef unsigned short u16;
typedef unsigned u32x4 __attribute__((ext_vector_type(4)));
typedef float f32x4 __attribute__((ext_vector_type(4)));
typedef __bf16 bf16x2 __attribute__((ext_vector_type(2)));

constexpr int B = 8, D = 576, HQ = 32, HKV = 16, SK = 4096, DH = 128, F = 4608;
constexpr int NSPLIT = 4;

__device__ __forceinline__ float bf2f(u16 v) { return __uint_as_float(((unsigned)v) << 16); }
__device__ __forceinline__ u16 f2bf(float f) { __hip_bfloat16 h = __float2bfloat16(f); return *reinterpret_cast<u16*>(&h); }
__device__ __forceinline__ float rbf(float f) {
  return f;
}
__device__ __forceinline__ float dot2(unsigned k, unsigned q, float acc) {
  return __builtin_amdgcn_fdot2_f32_bf16(__builtin_bit_cast(bf16x2, k), __builtin_bit_cast(bf16x2, q), acc, false);
}
template <int CTRL> __device__ __forceinline__ float dpp(float x) {
  return __builtin_bit_cast(float, __builtin_amdgcn_mov_dpp(__builtin_bit_cast(int, x), CTRL, 0xf, 0xf, true));
}
constexpr int XOR1 = 0xB1, XOR2 = 0x4E, XOR7 = 0x141, XOR8 = 0x128, ROR4 = 0x124, ROR2 = 0x122;
__device__ __forceinline__ float xrow16_max(float x) {
  auto s = __builtin_amdgcn_permlane16_swap(__float_as_uint(x), __float_as_uint(x), false, false);
  x = fmaxf(__uint_as_float(s[0]), __uint_as_float(s[1]));
  auto t = __builtin_amdgcn_permlane32_swap(__float_as_uint(x), __float_as_uint(x), false, false);
  return fmaxf(__uint_as_float(t[0]), __uint_as_float(t[1]));
}
__device__ __forceinline__ float xrow16_sum(float x) {
  auto s = __builtin_amdgcn_permlane16_swap(__float_as_uint(x), __float_as_uint(x), false, false);
  x = __uint_as_float(s[0]) + __uint_as_float(s[1]);
  auto t = __builtin_amdgcn_permlane32_swap(__float_as_uint(x), __float_as_uint(x), false, false);
  return __uint_as_float(t[0]) + __uint_as_float(t[1]);
}

__device__ void ln_rows(const float* xin, float* xn) {
  const int tid = threadIdx.x, lane = tid & 63, wave = tid >> 6;
  for (int rr = 0; rr < 2; ++rr) {
    const int row = wave * 2 + rr;
    float v[9]; float s = 0.f;
    for (int i = 0; i < 9; ++i) { v[i] = xin[row * D + lane + 64 * i]; s += v[i]; }
    for (int o = 32; o >= 1; o >>= 1) s += __shfl_xor(s, o);
    const float mean = s * (1.f / D);
    float q = 0.f;
    for (int i = 0; i < 9; ++i) { const float d = v[i] - mean; q += d * d; }
    for (int o = 32; o >= 1; o >>= 1) q += __shfl_xor(q, o);
    const float var = q * (1.f / D);
    const float mb = rbf(mean), vb = rbf(var);
    const float sd = rbf(sqrtf(rbf(vb + 1e-5f)));
    for (int i = 0; i < 9; ++i) xn[row * D + lane + 64 * i] = rbf(rbf(v[i] - mb) / sd);
  }
}

__global__ void __launch_bounds__(256) k_qproj(const u16* x, const u16* wq, u16* q) {
  __shared__ float xs[B * D], xn[B * D], red[16][B][16];
  const int tid = threadIdx.x;
  for (int i = tid; i < B * D; i += 256) xs[i] = bf2f(x[i]);
  __syncthreads();
  ln_rows(xs, xn);
  __syncthreads();
  const int col = blockIdx.x * 16 + (tid & 15), ks = tid >> 4;
  float acc[B];
  for (int b = 0; b < B; ++b) acc[b] = 0.f;
  for (int k = ks * 36; k < ks * 36 + 36; ++k) {
    const float w = bf2f(wq[(size_t)k * (HQ * DH) + col]);
    for (int b = 0; b < B; ++b) acc[b] = fmaf(xn[b * D + k], w, acc[b]);
  }
  for (int b = 0; b < B; ++b) red[ks][b][tid & 15] = acc[b];
  __syncthreads();
  if (tid < 128) {
    const int b = tid >> 4, c = tid & 15;
    float s = 0.f;
    for (int i = 0; i < 16; ++i) s += red[i][b][c];
    q[b * (HQ * DH) + blockIdx.x * 16 + c] = f2bf(s);
  }
}

__global__ void __launch_bounds__(256) k_attn(const u16* q, const u16* kc, const u16* vc, float* pml, float* po) {
  __shared__ __attribute__((aligned(16))) float lds[4 * 64 + 4 * 2 * (4 + DH)];
  const int s = blockIdx.x, kvh = blockIdx.y, b = blockIdx.z;
  const int tid = threadIdx.x, lane = tid & 63, wave = __builtin_amdgcn_readfirstlane(tid >> 6), r = lane >> 4, c = lane & 15;
  constexpr int KPS = SK / NSPLIT;
  constexpr int NIT = KPS / (32 * 4);
  const float SCALE = 0.088388347648318440f;
  const float LOG2E = 1.4426950408889634f;
  u32x4 qpk[2];
  for (int g = 0; g < 2; ++g) qpk[g] = *reinterpret_cast<const u32x4*>(q + (size_t)b * HQ * DH + (kvh * 2 + g) * DH + 8 * c);
  const size_t base = ((size_t)(b * HKV + kvh) * SK + (size_t)s * KPS) * DH + (size_t)r * DH + 8 * c;
  u32x4 Kn[8], Vn[8];
  auto issue = [&](int it) {
    const size_t o = base + (size_t)(it * 4 + wave) * 32 * DH;
#pragma unroll
    for (int t = 0; t < 8; ++t) Kn[t] = __builtin_nontemporal_load(reinterpret_cast<const u32x4*>(kc + o + (size_t)t * 4 * DH));
#pragma unroll
    for (int t = 0; t < 8; ++t) Vn[t] = __builtin_nontemporal_load(reinterpret_cast<const u32x4*>(vc + o + (size_t)t * 4 * DH));
  };
  float m = -INFINITY, l = 0.f, acc[16];
#pragma unroll
  for (int k = 0; k < 16; ++k) acc[k] = 0.f;
  issue(0);
  for (int it = 0; it < NIT; ++it) {
    u32x4 Kc[8], Vc[8];
#pragma unroll
    for (int t = 0; t < 8; ++t) { Kc[t] = Kn[t]; Vc[t] = Vn[t]; }
    if (it + 1 < NIT) issue(it + 1);
    float x[16];
#pragma unroll
    for (int t = 0; t < 8; ++t)
#pragma unroll
      for (int g = 0; g < 2; ++g) { float d = 0.f;
#pragma unroll
        for (int k = 0; k < 4; ++k) d = dot2(Kc[t][k], qpk[g][k], d);
        x[t * 2 + g] = d; }
#define TR_STEP(HALF, CTRL, BIT) _Pragma("unroll") for (int h = 0; h < (HALF); ++h) { \
      const float lo_ = x[h], hi_ = x[h + (HALF)]; const float t1_ = lo_ + dpp<CTRL>(lo_), t2_ = hi_ + dpp<CTRL>(hi_); \
      x[h] = (c & (BIT)) ? t2_ : t1_; }
    TR_STEP(8, XOR8, 8) TR_STEP(4, XOR7, 4) TR_STEP(2, XOR2, 2) TR_STEP(1, XOR1, 1)
#undef TR_STEP
    const float sc = rbf(rbf(x[0]) * SCALE) * LOG2E;
    float pm = sc; pm = fmaxf(pm, dpp<ROR2>(pm)); pm = fmaxf(pm, dpp<ROR4>(pm)); pm = fmaxf(pm, dpp<XOR8>(pm)); pm = xrow16_max(pm);
    const float mn = fmaxf(m, pm);
    const float alpha = __builtin_amdgcn_exp2f(m - mn), p = __builtin_amdgcn_exp2f(sc - mn);
    l = fmaf(l, alpha, p); m = mn;
    const float a0 = dpp<0x00>(alpha), a1 = dpp<0x55>(alpha);
#pragma unroll
    for (int k = 0; k < 8; ++k) { acc[k] *= a0; acc[8 + k] *= a1; }
    float* prow = lds + (wave * 4 + r) * 16;
    prow[c] = p;
    asm volatile("s_waitcnt lgkmcnt(0)" ::: "memory");
    f32x4 pr[4];
#pragma unroll
    for (int u = 0; u < 4; ++u) pr[u] = *reinterpret_cast<const f32x4*>(prow + 4 * u);
#pragma unroll
    for (int t = 0; t < 8; ++t) {
      float vf[8];
#pragma unroll
      for (int k = 0; k < 4; ++k) { vf[2 * k] = __builtin_bit_cast(float, Vc[t][k] << 16); vf[2 * k + 1] = __builtin_bit_cast(float, Vc[t][k] & 0xffff0000u); }
#pragma unroll
      for (int g = 0; g < 2; ++g) { const float pg = pr[(t * 2 + g) >> 2][(t * 2 + g) & 3];
#pragma unroll
        for (int k = 0; k < 8; ++k) acc[g * 8 + k] = fmaf(pg, vf[k], acc[g * 8 + k]); } }
  }
#pragma unroll
  for (int k = 0; k < 16; ++k) acc[k] = xrow16_sum(acc[k]);
  { float t = l; t += dpp<ROR2>(t); t += dpp<ROR4>(t); t += dpp<XOR8>(t); l = xrow16_sum(t); }
  float* mrg = lds + 4 * 64;
  if (r == 0) {
#pragma unroll
    for (int g = 0; g < 2; ++g) {
      float* rec = mrg + (wave * 2 + g) * (4 + DH);
#pragma unroll
      for (int k = 0; k < 8; ++k) rec[4 + 8 * c + k] = acc[g * 8 + k];
      if (c == g) { rec[0] = m; rec[1] = l; } } }
  __syncthreads();
  {
    const int g = tid >> 7, d = tid & 127;
    float M = -INFINITY;
    for (int w = 0; w < 4; ++w) M = fmaxf(M, mrg[(w * 2 + g) * (4 + DH)]);
    float L = 0.f, o = 0.f;
    for (int w = 0; w < 4; ++w) { const float* rec = mrg + (w * 2 + g) * (4 + DH); const float wt = __builtin_amdgcn_exp2f(rec[0] - M);
      L = fmaf(wt, rec[1], L); o = fmaf(wt, rec[4 + d], o); }
    const size_t slot = ((size_t)b * HQ + kvh * 2 + g) * NSPLIT + s;
    po[slot * DH + d] = o;
    if (d == 0) { pml[slot * 2] = M; pml[slot * 2 + 1] = L; }
  }
}

__global__ void __launch_bounds__(256) k_wo(const float* pml, const float* po, const u16* wo, const u16* x, float* h) {
  __shared__ float o[HQ * DH], red[4][64];
  const int tid = threadIdx.x, b = blockIdx.y, cb = blockIdx.x;
  for (int i = tid; i < HQ * DH; i += 256) {
    const int hh = i >> 7, d = i & 127;
    const size_t s0 = ((size_t)b * HQ + hh) * NSPLIT;
    float M = -INFINITY;
    for (int s = 0; s < NSPLIT; ++s) M = fmaxf(M, pml[(s0 + s) * 2]);
    float L = 0.f, a = 0.f;
    for (int s = 0; s < NSPLIT; ++s) { const float wt = __builtin_amdgcn_exp2f(pml[(s0 + s) * 2] - M); L = fmaf(wt, pml[(s0 + s) * 2 + 1], L); a = fmaf(wt, po[(s0 + s) * DH + d], a); }
    o[i] = rbf(a / L);
  }
  __syncthreads();
  const int col = cb * 64 + (tid & 63), ks = tid >> 6;
  float acc = 0.f;
  for (int k = ks * 1024; k < ks * 1024 + 1024; ++k) acc = fmaf(o[k], bf2f(wo[(size_t)k * D + col]), acc);
  red[ks][tid & 63] = acc;
  __syncthreads();
  if (tid < 64) {
    const float s = red[0][tid] + red[1][tid] + red[2][tid] + red[3][tid];
    h[b * D + col] = rbf(bf2f(x[b * D + col]) + rbf(s));
  }
}

__device__ __forceinline__ float gelu_tanh(float x) {
  const float k0 = 0.7978845608028654f, k1 = 0.044715f;
  const float u = k0 * (x + k1 * x * x * x);
  return 0.5f * x * (1.f + tanhf(u));
}

__global__ void __launch_bounds__(256) k_gateup(const float* h, const u16* wg, const u16* wu, float* act) {
  __shared__ float xs[B * D], xn[B * D], red[4][2][B][64];
  const int tid = threadIdx.x;
  for (int i = tid; i < B * D; i += 256) xs[i] = h[i];
  __syncthreads();
  ln_rows(xs, xn);
  __syncthreads();
  const int col = blockIdx.x * 64 + (tid & 63), ks = tid >> 6;
  float ag[B], au[B];
  for (int b = 0; b < B; ++b) { ag[b] = 0.f; au[b] = 0.f; }
  for (int k = ks * 144; k < ks * 144 + 144; ++k) {
    const float g = bf2f(wg[(size_t)k * F + col]), u = bf2f(wu[(size_t)k * F + col]);
    for (int b = 0; b < B; ++b) { ag[b] = fmaf(xn[b * D + k], g, ag[b]); au[b] = fmaf(xn[b * D + k], u, au[b]); }
  }
  for (int b = 0; b < B; ++b) { red[ks][0][b][tid & 63] = ag[b]; red[ks][1][b][tid & 63] = au[b]; }
  __syncthreads();
  for (int i = tid; i < B * 64; i += 256) {
    const int b = i >> 6, c = i & 63;
    const float g = rbf(red[0][0][b][c] + red[1][0][b][c] + red[2][0][b][c] + red[3][0][b][c]);
    const float u = rbf(red[0][1][b][c] + red[1][1][b][c] + red[2][1][b][c] + red[3][1][b][c]);
    act[b * F + blockIdx.x * 64 + c] = rbf(rbf(gelu_tanh(g)) * u);
  }
}

__global__ void __launch_bounds__(256) k_down(const float* act, const u16* wd, const float* h, u16* out) {
  __shared__ float a[F], red[4][64];
  const int tid = threadIdx.x, b = blockIdx.y, cb = blockIdx.x;
  for (int i = tid; i < F; i += 256) a[i] = act[b * F + i];
  __syncthreads();
  const int col = cb * 64 + (tid & 63), ks = tid >> 6;
  float acc = 0.f;
  for (int k = ks * 1152; k < ks * 1152 + 1152; ++k) acc = fmaf(a[k], bf2f(wd[(size_t)k * D + col]), acc);
  red[ks][tid & 63] = acc;
  __syncthreads();
  if (tid < 64) {
    const float s = red[0][tid] + red[1][tid] + red[2][tid] + red[3][tid];
    out[b * D + col] = f2bf(h[b * D + col] + rbf(s));
  }
}

extern "C" void kernel_launch(void* const* d_in, const int* in_sizes, int n_in,
                              void* d_out, int out_size, void* d_ws, size_t ws_size,
                              hipStream_t stream) {
  const u16* x = (const u16*)d_in[0];
  const u16* kc = (const u16*)d_in[1];
  const u16* vc = (const u16*)d_in[2];
  const u16* wq = (const u16*)d_in[3];
  const u16* wo = (const u16*)d_in[4];
  const u16* wg = (const u16*)d_in[6];
  const u16* wu = (const u16*)d_in[7];
  const u16* wd = (const u16*)d_in[8];
  char* ws = (char*)d_ws;
  u16* q = (u16*)ws;
  float* pml = (float*)(ws + 65536);
  float* po = (float*)(ws + 131072);
  float* h = (float*)(ws + 131072 + 4194304);
  float* act = (float*)(ws + 131072 + 4194304 + 65536);
  k_qproj<<<256, 256, 0, stream>>>(x, wq, q);
  k_attn<<<dim3(NSPLIT, HKV, B), 256, 0, stream>>>(q, kc, vc, pml, po);
  k_wo<<<dim3(9, B), 256, 0, stream>>>(pml, po, wo, x, h);
  k_gateup<<<72, 256, 0, stream>>>(h, wg, wu, act);
  k_down<<<dim3(9, B), 256, 0, stream>>>(act, wd, h, (u16*)d_out);
}
```

```cpp
#include <hip/hip_runtime.h>
#include <stdint.h>

#define LAS __attribute__((address_space(3)))
typedef unsigned short u16;
typedef unsigned long long u64;
typedef unsigned u32x4 __attribute__((ext_vector_type(4)));
typedef unsigned u32x2 __attribute__((ext_vector_type(2)));
typedef float f32x4 __attribute__((ext_vector_type(4)));
typedef float f32x2 __attribute__((ext_vector_type(2)));
typedef short s16x8 __attribute__((ext_vector_type(8)));
typedef short s16x4 __attribute__((ext_vector_type(4)));
typedef __bf16 bf16x2 __attribute__((ext_vector_type(2)));

constexpr int D = 576, SK = 4096, FF = 4608, NQ = 4096;
constexpr int NWG = 256, NTHR = 512;

constexpr int EPOCH_OFF = 0;
constexpr int CF_OFF = 4096;
constexpr int QG_OFF = 8192;
constexpr int AR_OFF = QG_OFF + 8 * NQ * 8;
constexpr int AR_STR = 1152;
constexpr int HP_OFF = AR_OFF + NWG * AR_STR;
constexpr int H_OFF = HP_OFF + 16 * 8 * D * 4;
constexpr int H_REP = 8 * D * 2;
constexpr int FP_OFF = H_OFF + 8 * H_REP;
constexpr int FP_STR = 8 * D * 4;
constexpr int WS_BYTES = FP_OFF + 192 * FP_STR;
constexpr int DIAG_OFF = 8 << 20;
constexpr unsigned SENT = 0xFFFFFFFFu;

constexpr int WQ_L = 0;
constexpr int TW_L = 18432;
constexpr int DN_L = TW_L + 55296;
constexpr int XN_L = TW_L + 82944;
constexpr int XN_STR = 1168;
constexpr int Q_L = XN_L + 8 * XN_STR;
constexpr int MG_L = Q_L + 512;
constexpr int MG_STR = 1040;
constexpr int LDS_BYTES = MG_L + 8 * MG_STR;
constexpr int OB_L = XN_L;
constexpr int OB_STR = 528;
constexpr int HB_L = WQ_L;
constexpr int ACT_L = WQ_L + 9216;
constexpr int ACT_STR = 80;
constexpr int R2_L = WQ_L + 10240;

constexpr unsigned SPIN_CAP = 1u << 18;

__device__ __forceinline__ float bflo(unsigned u) { return __uint_as_float(u << 16); }
__device__ __forceinline__ float bfhi(unsigned u) { return __uint_as_float(u & 0xffff0000u); }
__device__ __forceinline__ unsigned pkbf(float lo, float hi) { return __builtin_bit_cast(unsigned, __builtin_convertvector(f32x2{lo, hi}, bf16x2)); }
__device__ __forceinline__ float rbf(float x) { return bflo(pkbf(x, x)); }
__device__ __forceinline__ f32x4 mfma16(u32x4 a, u32x4 b, f32x4 c) {
  return __builtin_amdgcn_mfma_f32_16x16x32_bf16(__builtin_bit_cast(s16x8, a), __builtin_bit_cast(s16x8, b), c, 0, 0, 0);
}
template <int CTRL> __device__ __forceinline__ float dpp(float x) {
  return __builtin_bit_cast(float, __builtin_amdgcn_mov_dpp(__builtin_bit_cast(int, x), CTRL, 0xf, 0xf, true));
}
constexpr int XOR1 = 0xB1, XOR2 = 0x4E, XOR7 = 0x141, ROR8 = 0x128;
__device__ __forceinline__ float xrow_max(float x) {
  auto s = __builtin_amdgcn_permlane16_swap(__float_as_uint(x), __float_as_uint(x), false, false);
  x = fmaxf(__uint_as_float(s[0]), __uint_as_float(s[1]));
  auto t = __builtin_amdgcn_permlane32_swap(__float_as_uint(x), __float_as_uint(x), false, false);
  return fmaxf(__uint_as_float(t[0]), __uint_as_float(t[1]));
}
__device__ __forceinline__ float xrow_sum(float x) {
  auto s = __builtin_amdgcn_permlane16_swap(__float_as_uint(x), __float_as_uint(x), false, false);
  x = __uint_as_float(s[0]) + __uint_as_float(s[1]);
  auto t = __builtin_amdgcn_permlane32_swap(__float_as_uint(x), __float_as_uint(x), false, false);
  return __uint_as_float(t[0]) + __uint_as_float(t[1]);
}
__device__ __forceinline__ float row_sum(float x) {
  x += dpp<XOR1>(x); x += dpp<XOR2>(x); x += dpp<XOR7>(x); x += dpp<ROR8>(x); return x;
}
__device__ __forceinline__ float wave_sum(float x) { return xrow_sum(row_sum(x)); }

__device__ __forceinline__ void dma16(const void* gsrc, unsigned lds_dst) {
  unsigned keep;
  asm volatile("s_mov_b32 %0, m0\n\ts_mov_b32 m0, %2\n\ts_nop 0\n\tglobal_load_lds_dwordx4 %1, off nt\n\ts_mov_b32 m0, %0"
               : "=&s"(keep) : "v"(gsrc), "s"(lds_dst) : "memory");
}
#define WG_BARRIER() do { asm volatile("s_waitcnt lgkmcnt(0)" ::: "memory"); __builtin_amdgcn_s_barrier(); asm volatile("" ::: "memory"); } while (0)
#define VMWAIT(n) asm volatile("s_waitcnt vmcnt(" #n ")" ::: "memory")
#define MEMFENCE() asm volatile("" ::: "memory")

typedef __amdgpu_buffer_rsrc_t rsrc_t;
__device__ __forceinline__ u32x4 ld128(rsrc_t rs, int off) { return __builtin_amdgcn_raw_buffer_load_b128(rs, off, 0, 16); }
__device__ __forceinline__ u32x2 ld64(rsrc_t rs, int off) { return __builtin_amdgcn_raw_buffer_load_b64(rs, off, 0, 16); }
__device__ __forceinline__ void st128(rsrc_t rs, int off, u32x4 v) { __builtin_amdgcn_raw_buffer_store_b128(v, rs, off, 0, 16); }
__device__ __forceinline__ void st64(rsrc_t rs, int off, u32x2 v) { __builtin_amdgcn_raw_buffer_store_b64(v, rs, off, 0, 16); }
__device__ __forceinline__ unsigned canon(float v) { const unsigned u = __float_as_uint(v); return u < 0xFFFFFFFEu ? u : 0xFFFFFFFEu; }
__device__ __forceinline__ unsigned canonu(unsigned u) { return u < 0xFFFFFFFEu ? u : 0xFFFFFFFEu; }
__device__ __forceinline__ u32x4 canon4(f32x4 v) { return u32x4{canon(v[0]), canon(v[1]), canon(v[2]), canon(v[3])}; }
__device__ __forceinline__ bool ok4(u32x4 v) { return v[0] != SENT && v[1] != SENT && v[2] != SENT && v[3] != SENT; }
__device__ __forceinline__ bool ok2(u32x2 v) { return v[0] != SENT && v[1] != SENT; }

__device__ __forceinline__ u32x4 lds128(LAS unsigned char* L, int off) { return *(LAS u32x4*)(L + off); }
__device__ __forceinline__ u32x4 tr_frag(LAS unsigned char* L, int off, int off2) {
  const s16x4 a = __builtin_amdgcn_ds_read_tr16_b64_v4i16((LAS s16x4*)(L + off));
  const s16x4 b = __builtin_amdgcn_ds_read_tr16_b64_v4i16((LAS s16x4*)(L + off2));
  const u32x2 x = __builtin_bit_cast(u32x2, a), y = __builtin_bit_cast(u32x2, b);
  return u32x4{x[0], x[1], y[0], y[1]};
}

__device__ __forceinline__ void ln_row(u32x4 xa, u32x4 xb, int lane, LAS unsigned char* dst) {
  float v[8], u[8];
#pragma unroll
  for (int i = 0; i < 4; ++i) { v[2 * i] = bflo(xa[i]); v[2 * i + 1] = bfhi(xa[i]); u[2 * i] = bflo(xb[i]); u[2 * i + 1] = bfhi(xb[i]); }
  const bool tail = lane < 8;
  float s0 = ((v[0] + v[1]) + (v[2] + v[3])) + ((v[4] + v[5]) + (v[6] + v[7]));
  float s1 = ((u[0] + u[1]) + (u[2] + u[3])) + ((u[4] + u[5]) + (u[6] + u[7]));
  const float mean = wave_sum(s0 + (tail ? s1 : 0.f)) * (1.f / D);
  float q0 = 0.f, q1 = 0.f;
#pragma unroll
  for (int i = 0; i < 8; ++i) { const float a = v[i] - mean, c = u[i] - mean; q0 = fmaf(a, a, q0); q1 = fmaf(c, c, q1); }
  const float var = wave_sum(q0 + (tail ? q1 : 0.f)) * (1.f / D);
  const float mb = rbf(mean);
  const float sd = rbf(__builtin_sqrtf(rbf(rbf(var) + 1e-5f)));
  const float r = 1.0f / sd;
  u32x4 ya, yb;
#pragma unroll
  for (int i = 0; i < 4; ++i) {
    const unsigned da = pkbf(v[2 * i] - mb, v[2 * i + 1] - mb), db = pkbf(u[2 * i] - mb, u[2 * i + 1] - mb);
    ya[i] = pkbf(bflo(da) * r, bfhi(da) * r); yb[i] = pkbf(bflo(db) * r, bfhi(db) * r);
  }
  *(LAS u32x4*)(dst + lane * 16) = ya;
  if (tail) *(LAS u32x4*)(dst + 1024 + lane * 16) = yb;
}

#define STAMP(i) do {} while (0)

__global__ void __launch_bounds__(NTHR, 2)
fused_block(const u16* __restrict__ x, const u16* __restrict__ kc, const u16* __restrict__ vc, const u16* __restrict__ wq,
            const u16* __restrict__ wo, const u16* __restrict__ wg, const u16* __restrict__ wu, const u16* __restrict__ wd,
            u16* __restrict__ out, unsigned char* __restrict__ ws) {
  __shared__ __attribute__((aligned(16))) unsigned char lds_raw[LDS_BYTES];
  LAS unsigned char* L = (LAS unsigned char*)lds_raw;
  const unsigned lbase = (unsigned)(size_t)L;
  const int tid = threadIdx.x, lane = tid & 63, w = __builtin_amdgcn_readfirstlane(tid >> 6);
  const int bid = blockIdx.x, xcd = bid & 7, jj = bid >> 3, kvh = 2 * xcd + (jj >> 4), mem = jj & 15, b = mem >> 1, split = mem & 1;
  const int a = lane >> 4, n = lane & 15;
  const bool is_wo = mem < 4;
  const int ns = mem, f = kvh * 12 + (mem - 4);
  const rsrc_t rs = __builtin_amdgcn_make_buffer_rsrc((void*)ws, 0, WS_BYTES, 0x00020000);
  if (w == 0) STAMP(0);

  {
    const u16* src = wq + (size_t)(lane >> 1) * NQ + kvh * 256 + mem * 16 + (lane & 1) * 8;
    dma16(src + (size_t)w * 32 * NQ, lbase + WQ_L + w * 1024);
    dma16(src + (size_t)(w + 8) * 32 * NQ, lbase + WQ_L + (w + 8) * 1024);
    if (w < 2) dma16(src + (size_t)(w + 16) * 32 * NQ, lbase + WQ_L + (w + 16) * 1024);
  }
  const u32x2 ep = ld64(rs, EPOCH_OFF);
  const u32x4 xa = *(const u32x4*)(x + w * D + lane * 8);
  const u32x4 xb = *(const u32x4*)(x + w * D + 512 + (lane & 7) * 8);
  const size_t kvoff = ((size_t)(b * 16 + kvh) * SK + split * 2048 + w * 16) * 256;
  const char* kp = (const char*)kc + kvoff + (4 * (n & 3) + (n >> 2)) * 256 + a * 16;
  const char* vp = (const char*)vc + kvoff + lane * 16;
  u32x4 Kf[4], Vf[4];
#pragma unroll
  for (int s = 0; s < 4; ++s) Kf[s] = __builtin_nontemporal_load((const u32x4*)(kp + 64 * s));
  MEMFENCE();
  ln_row(xa, xb, lane, L + XN_L + w * XN_STR);
  const u64 tag = (((u64)ep[1] << 32) | ep[0]) + 1ull;
  const unsigned tag_lo = (unsigned)tag, tag_hi = (unsigned)(tag >> 32);
  const unsigned gt_lo = (tag_lo & 0xffffu) << 16, gt_hi = (unsigned)(tag >> 16);
  WG_BARRIER();
  if (w == 0) {
    STAMP(1);
    f32x4 acc = {0.f, 0.f, 0.f, 0.f};
    const int aoff = WQ_L + (8 * a + (n >> 2)) * 32 + (n & 3) * 8;
    const int boff = XN_L + (n & 7) * XN_STR + a * 16;
#pragma unroll
    for (int s = 0; s < 18; ++s) acc = mfma16(tr_frag(L, aoff + s * 1024, aoff + s * 1024 + 128), lds128(L, boff + s * 64), acc);
    const unsigned pk = (n < 8) ? pkbf(acc[0], acc[1]) : pkbf(acc[2], acc[3]);
    st128(rs, QG_OFF + ((n & 7) * NQ + kvh * 256 + mem * 16 + 4 * a + 2 * (n >> 3)) * 8,
          u32x4{gt_lo | (pk & 0xffffu), gt_hi, gt_lo | (pk >> 16), gt_hi});
    MEMFENCE();
    STAMP(2);
#pragma unroll
    for (int j = 0; j < 4; ++j) Vf[j] = __builtin_nontemporal_load((const u32x4*)(vp + 1024 * j));
    const int qoff = QG_OFF + (b * NQ + kvh * 256 + 4 * lane) * 8;
    u32x4 g0, g1;
    for (unsigned spins = 0;; ++spins) {
      MEMFENCE();
      g0 = ld128(rs, qoff); g1 = ld128(rs, qoff + 16);
      const bool ok = ((g0[0] ^ gt_lo) >> 16) == 0 && g0[1] == gt_hi && ((g0[2] ^ gt_lo) >> 16) == 0 && g0[3] == gt_hi &&
                      ((g1[0] ^ gt_lo) >> 16) == 0 && g1[1] == gt_hi && ((g1[2] ^ gt_lo) >> 16) == 0 && g1[3] == gt_hi;
      if (__all(ok) || spins > SPIN_CAP) break;
      __builtin_amdgcn_s_sleep(1);
    }
    *(LAS u32x2*)(L + Q_L + lane * 8) = u32x2{(g0[0] & 0xffffu) | (g0[2] << 16), (g1[0] & 0xffffu) | (g1[2] << 16)};
    STAMP(3);
  } else {
    const int w1 = w - 1;
    const u32x4 sent = {SENT, SENT, SENT, SENT};
    if (w1 == 0) { st128(rs, AR_OFF + bid * AR_STR + lane * 16, sent); if (lane == 0) st128(rs, AR_OFF + bid * AR_STR + 1024, sent); }
    if (is_wo) {
      const int c = w1 * 64 + lane;
      if (c < 288) st128(rs, HP_OFF + ((kvh * 8 + c / 36) * D + ns * 144) * 4 + (c % 36) * 16, sent);
      else if (c < 288 + 72) { const int e = c - 288; st128(rs, H_OFF + (e / 9) * H_REP + ((kvh >> 1) * D + ns * 144 + (kvh & 1) * 72) * 2 + (e % 9) * 16, sent); }
    } else {
#pragma unroll
      for (int t = 0; t < 3; ++t) { const int i = w1 + 7 * t; if (i < 18) st128(rs, FP_OFF + f * FP_STR + i * 1024 + lane * 16, sent); }
    }
    MEMFENCE();
#pragma unroll
    for (int t = 0; t < 12; ++t) {
      const int i = w1 + 7 * t;
      const int ci = i * 64 + lane;
      if (is_wo) {
        if (i < 72) { const int row = ci / 18, e = ci - row * 18; dma16(wo + (size_t)(kvh * 256 + row) * D + ns * 144 + e * 8, lbase + TW_L + i * 1024); }
      } else if (i < 54) {
        const int row = ci / 6, e = ci - row * 6;
        const u16* src = (e < 3 ? wg : wu) + (size_t)row * FF + f * 24 + (e < 3 ? e : e - 3) * 8;
        dma16(src, lbase + TW_L + i * 1024);
      } else if (i < 81) {
        dma16(wd + (size_t)f * 24 * D + (i - 54) * 512 + lane * 8, lbase + TW_L + i * 1024);
      }
      if ((t & 3) == 3) VMWAIT(4);
    }
#pragma unroll
    for (int j = 0; j < 4; ++j) Vf[j] = __builtin_nontemporal_load((const u32x4*)(vp + 1024 * j));
    MEMFENCE();
  }
  WG_BARRIER();
  if (w == 1 && lane == 0) st64(rs, CF_OFF + bid * 8, u32x2{tag_lo, tag_hi});
  u32x4 Qf[4];
#pragma unroll
  for (int s = 0; s < 4; ++s) Qf[s] = lds128(L, Q_L + (n >> 3) * 256 + s * 64 + a * 16);
  u32x4 pmask;
#pragma unroll
  for (int i = 0; i < 4; ++i) pmask[i] = ((lane & 7) >> 1) == i ? ((lane & 1) ? 0xffff0000u : 0x0000ffffu) : 0u;
  const bool own_head = (lane >> 5) == ((lane >> 3) & 1);

  const float SCALE = 0.088388347648318440f, LOG2E = 1.4426950408889634f;
  float M = -INFINITY, ML = -INFINITY, lsum = 0.f;
  f32x4 O = {0.f, 0.f, 0.f, 0.f};
  auto step = [&](int it) {
    f32x4 S = {0.f, 0.f, 0.f, 0.f};
#pragma unroll
    for (int s = 0; s < 4; ++s) S = mfma16(Kf[s], Qf[s], S);
    if (it + 1 < 16) {
#pragma unroll
      for (int s = 0; s < 4; ++s) Kf[s] = __builtin_nontemporal_load((const u32x4*)(kp + (size_t)(it + 1) * 32768 + 64 * s));
    }
    MEMFENCE();
    const unsigned r0 = pkbf(S[0], S[1]), r1 = pkbf(S[2], S[3]);
    const unsigned t0 = pkbf(bflo(r0) * SCALE, bfhi(r0) * SCALE), t1 = pkbf(bflo(r1) * SCALE, bfhi(r1) * SCALE);
    float sc[4] = {bflo(t0), bfhi(t0), bflo(t1), bfhi(t1)};
    float mx = fmaxf(fmaxf(sc[0], sc[1]), fmaxf(sc[2], sc[3]));
    if (__any(mx > M)) {
      mx = xrow_max(mx);
      const float Mn = fmaxf(M, mx), MLn = Mn * LOG2E;
      const float alpha = __builtin_amdgcn_exp2f(ML - MLn);
      const float al2 = dpp<ROR8>(alpha);
      const float ao = own_head ? alpha : al2;
      lsum *= alpha; O *= ao; M = Mn; ML = MLn;
    }
#pragma unroll
    for (int j = 0; j < 4; ++j) {
      const float p = __builtin_amdgcn_exp2f(fmaf(sc[j], LOG2E, -ML));
      lsum += p;
      const unsigned pk = pkbf(p, p);
      O = mfma16(u32x4{pk & pmask[0], pk & pmask[1], pk & pmask[2], pk & pmask[3]}, Vf[j], O);
    }
    if (it + 1 < 16) {
#pragma unroll
      for (int j = 0; j < 4; ++j) Vf[j] = __builtin_nontemporal_load((const u32x4*)(vp + (size_t)(it + 1) * 32768 + 1024 * j));
    }
    MEMFENCE();
  };
  step(0);
#pragma unroll 1
  for (int it = 1; it < 8; ++it) step(it);
  u32x4 c0 = {0, 0, 0, 0}, c1 = {0, 0, 0, 0};
  if (w == 7) { c0 = ld128(rs, CF_OFF + lane * 32); c1 = ld128(rs, CF_OFF + lane * 32 + 16); }
  MEMFENCE();
  step(8);
  if (w == 7) {
    auto cf_ok = [&](u32x4 p, u32x4 q) { return p[0] == tag_lo && p[1] == tag_hi && p[2] == tag_lo && p[3] == tag_hi &&
                                                q[0] == tag_lo && q[1] == tag_hi && q[2] == tag_lo && q[3] == tag_hi; };
    if (!__all(cf_ok(c0, c1))) {
      for (unsigned spins = 0;; ++spins) {
        __builtin_amdgcn_s_sleep(1);
        MEMFENCE();
        const u32x4 d0 = ld128(rs, CF_OFF + lane * 32), d1 = ld128(rs, CF_OFF + lane * 32 + 16);
        if (__all(cf_ok(d0, d1)) || spins > SPIN_CAP) break;
      }
    }
    if (bid == 0 && lane == 0) st64(rs, EPOCH_OFF, u32x2{tag_lo, tag_hi});
  }
#pragma unroll 1
  for (int it = 9; it < 15; ++it) step(it);
  step(15);
  if (w == 0) STAMP(4);
  lsum = xrow_sum(lsum);
  *(LAS f32x4*)(L + MG_L + w * MG_STR + ((a >> 1) * 128 + 8 * n + 4 * (a & 1)) * 4) = O;
  if ((lane & 55) == 0) *(LAS f32x2*)(L + MG_L + w * MG_STR + 1024 + (lane >> 3) * 8) = f32x2{ML, lsum};
  WG_BARRIER();
  if (w == 0) {
    STAMP(5);
    f32x2 ml[8]; f32x4 ow[8];
#pragma unroll
    for (int i = 0; i < 8; ++i) { ml[i] = *(LAS f32x2*)(L + MG_L + i * MG_STR + 1024 + (lane >> 5) * 8); ow[i] = *(LAS f32x4*)(L + MG_L + i * MG_STR + lane * 16); }
    float mxl = ml[0][0];
#pragma unroll
    for (int i = 1; i < 8; ++i) mxl = fmaxf(mxl, ml[i][0]);
    f32x4 o = {0.f, 0.f, 0.f, 0.f}; float ls = 0.f;
#pragma unroll
    for (int i = 0; i < 8; ++i) { const float wt = __builtin_amdgcn_exp2f(ml[i][0] - mxl); o += wt * ow[i]; ls = fmaf(wt, ml[i][1], ls); }
    st128(rs, AR_OFF + bid * AR_STR + lane * 16, canon4(o));
    if ((lane & 31) == 0) st64(rs, AR_OFF + bid * AR_STR + 1024 + (lane >> 5) * 8, u32x2{canon(mxl), canon(ls)});
    MEMFENCE();
    STAMP(6);
  }

  if (is_wo) {
    u32x4 A0[8], A1[8];
    const int aoff = TW_L + (8 * a + (n >> 2)) * 288 + (n & 3) * 8;
#pragma unroll
    for (int s = 0; s < 8; ++s) A0[s] = tr_frag(L, aoff + w * 32 + s * 32 * 288, aoff + w * 32 + s * 32 * 288 + 4 * 288);
    if (w == 7) {
#pragma unroll
      for (int s = 0; s < 8; ++s) A1[s] = tr_frag(L, aoff + 256 + s * 32 * 288, aoff + 256 + s * 32 * 288 + 4 * 288);
    }
    const int rb = kvh >> 1, rc = ns * 144 + (kvh & 1) * 72, ch = 4 * w + a;
    const bool rd = ch < 18;
    const u32x2 xr = *(const u32x2*)(x + rb * D + rc + (rd ? ch : 0) * 4);
    {
      const int r0 = AR_OFF + (xcd + 8 * ((jj & 16) + 2 * w)) * AR_STR, r1 = r0 + 8 * AR_STR;
      u32x4 o0, o1; u32x2 m0, m1;
      for (unsigned spins = 0;; ++spins) {
        MEMFENCE();
        o0 = ld128(rs, r0 + lane * 16); o1 = ld128(rs, r1 + lane * 16);
        m0 = ld64(rs, r0 + 1024 + (lane >> 5) * 8); m1 = ld64(rs, r1 + 1024 + (lane >> 5) * 8);
        if (__all(ok4(o0) && ok4(o1) && ok2(m0) && ok2(m1)) || spins > SPIN_CAP) break;
        __builtin_amdgcn_s_sleep(2);
      }
      const float ml0 = __uint_as_float(m0[0]), ml1 = __uint_as_float(m1[0]), mm = fmaxf(ml0, ml1);
      const float w0 = __builtin_amdgcn_exp2f(ml0 - mm), w1 = __builtin_amdgcn_exp2f(ml1 - mm);
      const float inv = 1.0f / (w0 * __uint_as_float(m0[1]) + w1 * __uint_as_float(m1[1]));
      const f32x4 o = (w0 * __builtin_bit_cast(f32x4, o0) + w1 * __builtin_bit_cast(f32x4, o1)) * inv;
      *(LAS u32x2*)(L + OB_L + w * OB_STR + lane * 8) = u32x2{pkbf(o[0], o[1]), pkbf(o[2], o[3])};
    }
    WG_BARRIER();
    if (w == 0) STAMP(7);
    {
      u32x4 Bf[8];
#pragma unroll
      for (int s = 0; s < 8; ++s) Bf[s] = lds128(L, OB_L + (n & 7) * OB_STR + s * 64 + a * 16);
      f32x4 acc = {0.f, 0.f, 0.f, 0.f};
#pragma unroll
      for (int s = 0; s < 8; ++s) acc = mfma16(A0[s], Bf[s], acc);
      const int hoff = HP_OFF + ((kvh * 8 + (n & 7)) * D + ns * 144 + 4 * a) * 4;
      if (w == 7) {
        f32x4 acc1 = {0.f, 0.f, 0.f, 0.f};
#pragma unroll
        for (int s = 0; s < 8; ++s) acc1 = mfma16(A1[s], Bf[s], acc1);
        st128(rs, hoff + (n < 8 ? 7 : 8) * 64, canon4(n < 8 ? acc : acc1));
      } else if (n < 8) st128(rs, hoff + w * 64, canon4(acc));
      MEMFENCE();
    }
    if (w == 0) STAMP(8);
    if (w < 5) {
      const int poff = HP_OFF + ((n * 8 + rb) * D + rc + (rd ? ch : 0) * 4) * 4;
      u32x4 pv;
      for (unsigned spins = 0;; ++spins) {
        MEMFENCE();
        pv = ld128(rs, poff);
        if (__all(ok4(pv)) || spins > SPIN_CAP) break;
        __builtin_amdgcn_s_sleep(1);
      }
      float s4[4];
#pragma unroll
      for (int i = 0; i < 4; ++i) s4[i] = row_sum(__uint_as_float(pv[i]));
      const unsigned a0 = pkbf(s4[0], s4[1]), a1 = pkbf(s4[2], s4[3]);
      const unsigned h0 = pkbf(bflo(xr[0]) + bflo(a0), bfhi(xr[0]) + bfhi(a0)), h1 = pkbf(bflo(xr[1]) + bflo(a1), bfhi(xr[1]) + bfhi(a1));
      if (rd && n < 8) st64(rs, H_OFF + n * H_REP + (rb * D + rc + ch * 4) * 2, u32x2{canonu(h0), canonu(h1)});
      if (w == 0) STAMP(9);
    }
    return;
  }

  u32x4 AG[18], AD[6];
  if (w < 3) {
    const int p = n & 3;
    const int aoff = TW_L + (8 * a + (n >> 2)) * 96 + (p >> 1) * 48 + w * 16 + (p & 1) * 8;
#pragma unroll
    for (int s = 0; s < 18; ++s) AG[s] = tr_frag(L, aoff + s * 32 * 96, aoff + s * 32 * 96 + 4 * 96);
  }
  {
    const int krow = (a < 3 ? 8 * a : 16) + (n >> 2);
    const int aoff = DN_L + krow * 1152 + (n & 3) * 8 + w * 64;
#pragma unroll
    for (int u = 0; u < 3; ++u) {
      if (u < 2 || w < 2) {
        AD[2 * u] = tr_frag(L, aoff + u * 512, aoff + u * 512 + 4 * 1152);
        AD[2 * u + 1] = tr_frag(L, aoff + u * 512 + 32, aoff + u * 512 + 32 + 4 * 1152);
      }
    }
  }
  if (w == 4 && lane < 8) *(LAS u32x4*)(L + ACT_L + lane * ACT_STR + 48) = u32x4{0, 0, 0, 0};
  {
    const int hoff = H_OFF + xcd * H_REP + w * D * 2;
    u32x4 ha, hb;
    for (unsigned spins = 0;; ++spins) {
      MEMFENCE();
      ha = ld128(rs, hoff + lane * 16); hb = ld128(rs, hoff + 1024 + (lane & 7) * 16);
      if (__all(ok4(ha) && ok4(hb)) || spins > SPIN_CAP) break;
      __builtin_amdgcn_s_sleep(4);
    }
    if (w == 0) STAMP(10);
    ln_row(ha, hb, lane, L + XN_L + w * XN_STR);
    *(LAS u32x4*)(L + HB_L + w * 1152 + lane * 16) = ha;
    if (lane < 8) *(LAS u32x4*)(L + HB_L + w * 1152 + 1024 + lane * 16) = hb;
  }
  WG_BARRIER();
  if (w == 0) STAMP(11);
  if (w < 3) {
    f32x4 acc = {0.f, 0.f, 0.f, 0.f};
    const int boff = XN_L + (n & 7) * XN_STR + a * 16;
#pragma unroll
    for (int s = 0; s < 18; ++s) acc = mfma16(AG[s], lds128(L, boff + s * 64), acc);
    const int j = 2 * (lane >> 5) + (n >> 3);
    const float keep = j == 0 ? acc[0] : j == 1 ? acc[1] : j == 2 ? acc[2] : acc[3];
    const int jo = j ^ 2;
    const float give = jo == 0 ? acc[0] : jo == 1 ? acc[1] : jo == 2 ? acc[2] : acc[3];
    auto sw = __builtin_amdgcn_permlane32_swap(__float_as_uint(give), __float_as_uint(give), false, false);
    const float got = __uint_as_float(lane < 32 ? sw[1] : sw[0]);
    const float g = rbf(lane < 32 ? keep : got), uu = rbf(lane < 32 ? got : keep);
    const float z = 0.7978845608028654f * (g + 0.044715f * g * g * g);
    const float ge = g * __builtin_amdgcn_rcpf(1.0f + __builtin_amdgcn_exp2f(-2.0f * 1.4426950408889634f * z));
    const float act = rbf(ge) * uu;
    *(LAS u16*)(L + ACT_L + (n & 7) * ACT_STR + (8 * w + 4 * (a & 1) + j) * 2) = (u16)(pkbf(act, act) & 0xffffu);
  }
  WG_BARRIER();
  if (w == 0) STAMP(12);
  {
    const u32x4 Bf = lds128(L, ACT_L + (n & 7) * ACT_STR + a * 16);
    const f32x4 z4 = {0.f, 0.f, 0.f, 0.f};
#pragma unroll
    for (int u = 0; u < 3; ++u) {
      if (u < 2 || w < 2) {
        const f32x4 d0 = mfma16(AD[2 * u], Bf, z4), d1 = mfma16(AD[2 * u + 1], Bf, z4);
        st128(rs, FP_OFF + f * FP_STR + ((n & 7) * D + (w + 8 * u) * 32 + (n >> 3) * 16 + 4 * a) * 4, canon4(n < 8 ? d0 : d1));
      }
    }
    MEMFENCE();
  }
  if (w == 0) STAMP(13);
  if ((f & 3) == 3) return;
  {
    const int r = (f >> 2) * 3 + (f & 3), rb = r / 18, c0 = (r - rb * 18) * 32;
    const int poff = FP_OFF + (tid >> 3) * FP_STR + (rb * D + c0 + (lane & 7) * 4) * 4;
    u32x4 p0, p1, p2;
    for (unsigned spins = 0;; ++spins) {
      MEMFENCE();
      p0 = ld128(rs, poff); p1 = ld128(rs, poff + 64 * FP_STR); p2 = ld128(rs, poff + 128 * FP_STR);
      if (__all(ok4(p0) && ok4(p1) && ok4(p2)) || spins > SPIN_CAP) break;
      __builtin_amdgcn_s_sleep(1);
    }
    if (w == 0) STAMP(14);
    f32x4 s = (__builtin_bit_cast(f32x4, p0) + __builtin_bit_cast(f32x4, p1)) + __builtin_bit_cast(f32x4, p2);
#pragma unroll
    for (int i = 0; i < 4; ++i) { float t = s[i]; t += dpp<ROR8>(t); s[i] = xrow_sum(t); }
    if (lane < 8) *(LAS f32x4*)(L + R2_L + w * 128 + lane * 16) = s;
    WG_BARRIER();
    if (w == 0 && lane < 8) {
      f32x4 t = {0.f, 0.f, 0.f, 0.f};
#pragma unroll
      for (int i = 0; i < 8; ++i) t += *(LAS f32x4*)(L + R2_L + i * 128 + lane * 16);
      const u32x2 hv = *(LAS u32x2*)(L + HB_L + rb * 1152 + (c0 + lane * 4) * 2);
      const unsigned y0 = pkbf(t[0], t[1]), y1 = pkbf(t[2], t[3]);
      *(u32x2*)(out + rb * D + c0 + lane * 4) =
          u32x2{pkbf(bflo(hv[0]) + bflo(y0), bfhi(hv[0]) + bfhi(y0)), pkbf(bflo(hv[1]) + bflo(y1), bfhi(hv[1]) + bfhi(y1))};
    }
    if (w == 0) STAMP(15);
  }
}

extern "C" void kernel_launch(void* const* d_in, const int* in_sizes, int n_in,
                              void* d_out, int out_size, void* d_ws, size_t ws_size,
                              hipStream_t stream) {
  fused_block<<<NWG, NTHR, 0, stream>>>((const u16*)d_in[0], (const u16*)d_in[1], (const u16*)d_in[2], (const u16*)d_in[3],
                                        (const u16*)d_in[4], (const u16*)d_in[6], (const u16*)d_in[7], (const u16*)d_in[8],
                                        (u16*)d_out, (unsigned char*)d_ws);
}
```

```cpp
#define VNT_BELOW 0
#define WPOL ""
#include <hip/hip_runtime.h>
#include <stdint.h>

#define LAS __attribute__((address_space(3)))
typedef unsigned short u16;
typedef unsigned long long u64;
typedef unsigned u32x4 __attribute__((ext_vector_type(4)));
typedef unsigned u32x2 __attribute__((ext_vector_type(2)));
typedef float f32x4 __attribute__((ext_vector_type(4)));
typedef float f32x2 __attribute__((ext_vector_type(2)));
typedef short s16x8 __attribute__((ext_vector_type(8)));
typedef short s16x4 __attribute__((ext_vector_type(4)));
typedef __bf16 bf16x2 __attribute__((ext_vector_type(2)));

constexpr int D = 576, SK = 4096, FF = 4608, NQ = 4096;
constexpr int NWG = 256, NTHR = 512;

constexpr int EPOCH_OFF = 0;
constexpr int CF_OFF = 4096;
constexpr int QG_OFF = 8192;
constexpr int AR_OFF = QG_OFF + 8 * NQ * 8;
constexpr int AR_STR = 1152;
constexpr int HP_OFF = AR_OFF + NWG * AR_STR;
constexpr int H_OFF = HP_OFF + 16 * 8 * D * 4;
constexpr int H_REP = 8 * D * 2;
constexpr int FP_OFF = H_OFF + 8 * H_REP;
constexpr int FP_STR = 8 * D * 4;
constexpr int WS_BYTES = FP_OFF + 192 * FP_STR;
constexpr int DIAG_OFF = 8 << 20;
constexpr unsigned SENT = 0xFFFFFFFFu;

constexpr int WQ_L = 0;
constexpr int TW_L = 18432;
constexpr int DN_L = TW_L + 55296;
constexpr int XN_L = TW_L + 82944;
constexpr int XN_STR = 1168;
constexpr int Q_L = XN_L + 8 * XN_STR;
constexpr int MG_L = Q_L + 512;
constexpr int MG_STR = 1040;
constexpr int LDS_BYTES = MG_L + 8 * MG_STR;
constexpr int OB_L = XN_L;
constexpr int OB_STR = 528;
constexpr int HB_L = WQ_L;
constexpr int ACT_L = WQ_L + 9216;
constexpr int ACT_STR = 80;
constexpr int R2_L = WQ_L + 10240;

constexpr unsigned SPIN_CAP = 1u << 18;

__device__ __forceinline__ float bflo(unsigned u) { return __uint_as_float(u << 16); }
__device__ __forceinline__ float bfhi(unsigned u) { return __uint_as_float(u & 0xffff0000u); }
__device__ __forceinline__ unsigned pkbf(float lo, float hi) { return __builtin_bit_cast(unsigned, __builtin_convertvector(f32x2{lo, hi}, bf16x2)); }
__device__ __forceinline__ float rbf(float x) { return bflo(pkbf(x, x)); }
__device__ __forceinline__ f32x4 mfma16(u32x4 a, u32x4 b, f32x4 c) {
  return __builtin_amdgcn_mfma_f32_16x16x32_bf16(__builtin_bit_cast(s16x8, a), __builtin_bit_cast(s16x8, b), c, 0, 0, 0);
}
template <int CTRL> __device__ __forceinline__ float dpp(float x) {
  return __builtin_bit_cast(float, __builtin_amdgcn_mov_dpp(__builtin_bit_cast(int, x), CTRL, 0xf, 0xf, true));
}
constexpr int XOR1 = 0xB1, XOR2 = 0x4E, XOR7 = 0x141, ROR8 = 0x128;
__device__ __forceinline__ float xrow_max(float x) {
  auto s = __builtin_amdgcn_permlane16_swap(__float_as_uint(x), __float_as_uint(x), false, false);
  x = fmaxf(__uint_as_float(s[0]), __uint_as_float(s[1]));
  auto t = __builtin_amdgcn_permlane32_swap(__float_as_uint(x), __float_as_uint(x), false, false);
  return fmaxf(__uint_as_float(t[0]), __uint_as_float(t[1]));
}
__device__ __forceinline__ float xrow_sum(float x) {
  auto s = __builtin_amdgcn_permlane16_swap(__float_as_uint(x), __float_as_uint(x), false, false);
  x = __uint_as_float(s[0]) + __uint_as_float(s[1]);
  auto t = __builtin_amdgcn_permlane32_swap(__float_as_uint(x), __float_as_uint(x), false, false);
  return __uint_as_float(t[0]) + __uint_as_float(t[1]);
}
__device__ __forceinline__ float row_sum(float x) {
  x += dpp<XOR1>(x); x += dpp<XOR2>(x); x += dpp<XOR7>(x); x += dpp<ROR8>(x); return x;
}
__device__ __forceinline__ float wave_sum(float x) { return xrow_sum(row_sum(x)); }

#define KNT 1
__device__ __forceinline__ u32x4 ldk(const char* p) { if (KNT) return __builtin_nontemporal_load((const u32x4*)p); else return *(const u32x4*)p; }
__device__ __forceinline__ void ldv4(u32x4 (&V)[4], const char* p, bool nt) {
  if (nt) {
#pragma unroll
    for (int j = 0; j < 4; ++j) V[j] = __builtin_nontemporal_load((const u32x4*)(p + 1024 * j));
  } else {
#pragma unroll
    for (int j = 0; j < 4; ++j) V[j] = *(const u32x4*)(p + 1024 * j);
  }
}
__device__ __forceinline__ void dma16(const void* gsrc, unsigned lds_dst) {
  unsigned keep;
  asm volatile("s_mov_b32 %0, m0\n\ts_mov_b32 m0, %2\n\ts_nop 0\n\tglobal_load_lds_dwordx4 %1, off" WPOL "\n\ts_mov_b32 m0, %0"
               : "=&s"(keep) : "v"(gsrc), "s"(lds_dst) : "memory");
}
#define WG_BARRIER() do { asm volatile("s_waitcnt lgkmcnt(0)" ::: "memory"); __builtin_amdgcn_s_barrier(); asm volatile("" ::: "memory"); } while (0)
#define VMWAIT(n) asm volatile("s_waitcnt vmcnt(" #n ")" ::: "memory")
#define MEMFENCE() asm volatile("" ::: "memory")

typedef __amdgpu_buffer_rsrc_t rsrc_t;
__device__ __forceinline__ u32x4 ld128(rsrc_t rs, int off) { return __builtin_amdgcn_raw_buffer_load_b128(rs, off, 0, 16); }
__device__ __forceinline__ u32x2 ld64(rsrc_t rs, int off) { return __builtin_amdgcn_raw_buffer_load_b64(rs, off, 0, 16); }
__device__ __forceinline__ void st128(rsrc_t rs, int off, u32x4 v) { __builtin_amdgcn_raw_buffer_store_b128(v, rs, off, 0, 16); }
__device__ __forceinline__ void st64(rsrc_t rs, int off, u32x2 v) { __builtin_amdgcn_raw_buffer_store_b64(v, rs, off, 0, 16); }
__device__ __forceinline__ unsigned canon(float v) { const unsigned u = __float_as_uint(v); return u < 0xFFFFFFFEu ? u : 0xFFFFFFFEu; }
__device__ __forceinline__ unsigned canonu(unsigned u) { return u < 0xFFFFFFFEu ? u : 0xFFFFFFFEu; }
__device__ __forceinline__ u32x4 canon4(f32x4 v) { return u32x4{canon(v[0]), canon(v[1]), canon(v[2]), canon(v[3])}; }
__device__ __forceinline__ bool ok4(u32x4 v) { return v[0] != SENT && v[1] != SENT && v[2] != SENT && v[3] != SENT; }
__device__ __forceinline__ bool ok2(u32x2 v) { return v[0] != SENT && v[1] != SENT; }

__device__ __forceinline__ u32x4 lds128(LAS unsigned char* L, int off) { return *(LAS u32x4*)(L + off); }
__device__ __forceinline__ u32x4 tr_frag(LAS unsigned char* L, int off, int off2) {
  const s16x4 a = __builtin_amdgcn_ds_read_tr16_b64_v4i16((LAS s16x4*)(L + off));
  const s16x4 b = __builtin_amdgcn_ds_read_tr16_b64_v4i16((LAS s16x4*)(L + off2));
  const u32x2 x = __builtin_bit_cast(u32x2, a), y = __builtin_bit_cast(u32x2, b);
  return u32x4{x[0], x[1], y[0], y[1]};
}

__device__ __forceinline__ void ln_row(u32x4 xa, u32x4 xb, int lane, LAS unsigned char* dst) {
  float v[8], u[8];
#pragma unroll
  for (int i = 0; i < 4; ++i) { v[2 * i] = bflo(xa[i]); v[2 * i + 1] = bfhi(xa[i]); u[2 * i] = bflo(xb[i]); u[2 * i + 1] = bfhi(xb[i]); }
  const bool tail = lane < 8;
  float s0 = ((v[0] + v[1]) + (v[2] + v[3])) + ((v[4] + v[5]) + (v[6] + v[7]));
  float s1 = ((u[0] + u[1]) + (u[2] + u[3])) + ((u[4] + u[5]) + (u[6] + u[7]));
  const float mean = wave_sum(s0 + (tail ? s1 : 0.f)) * (1.f / D);
  float q0 = 0.f, q1 = 0.f;
#pragma unroll
  for (int i = 0; i < 8; ++i) { const float a = v[i] - mean, c = u[i] - mean; q0 = fmaf(a, a, q0); q1 = fmaf(c, c, q1); }
  const float var = wave_sum(q0 + (tail ? q1 : 0.f)) * (1.f / D);
  const float mb = rbf(mean);
  const float sd = rbf(__builtin_sqrtf(rbf(rbf(var) + 1e-5f)));
  const float r = 1.0f / sd;
  u32x4 ya, yb;
#pragma unroll
  for (int i = 0; i < 4; ++i) {
    const unsigned da = pkbf(v[2 * i] - mb, v[2 * i + 1] - mb), db = pkbf(u[2 * i] - mb, u[2 * i + 1] - mb);
    ya[i] = pkbf(bflo(da) * r, bfhi(da) * r); yb[i] = pkbf(bflo(db) * r, bfhi(db) * r);
  }
  *(LAS u32x4*)(dst + lane * 16) = ya;
  if (tail) *(LAS u32x4*)(dst + 1024 + lane * 16) = yb;
}

#define STAMP(i) do {} while (0)

__global__ void __launch_bounds__(NTHR, 2)
fused_block(const u16* __restrict__ x, const u16* __restrict__ kc, const u16* __restrict__ vc, const u16* __restrict__ wq,
            const u16* __restrict__ wo, const u16* __restrict__ wg, const u16* __restrict__ wu, const u16* __restrict__ wd,
            u16* __restrict__ out, unsigned char* __restrict__ ws) {
  __shared__ __attribute__((aligned(16))) unsigned char lds_raw[LDS_BYTES];
  LAS unsigned char* L = (LAS unsigned char*)lds_raw;
  const unsigned lbase = (unsigned)(size_t)L;
  const int tid = threadIdx.x, lane = tid & 63, w = __builtin_amdgcn_readfirstlane(tid >> 6);
  const int bid = blockIdx.x, xcd = bid & 7, jj = bid >> 3, kvh = 2 * xcd + (jj >> 4), mem = jj & 15, b = mem >> 1, split = mem & 1;
  const int a = lane >> 4, n = lane & 15;
  const bool is_wo = mem < 4;
  const bool vnt = b < VNT_BELOW;
  const int ns = mem, f = kvh * 12 + (mem - 4);
  const rsrc_t rs = __builtin_amdgcn_make_buffer_rsrc((void*)ws, 0, WS_BYTES, 0x00020000);
  if (w == 0) STAMP(0);

  {
    const u16* src = wq + (size_t)(lane >> 1) * NQ + kvh * 256 + mem * 16 + (lane & 1) * 8;
    dma16(src + (size_t)w * 32 * NQ, lbase + WQ_L + w * 1024);
    dma16(src + (size_t)(w + 8) * 32 * NQ, lbase + WQ_L + (w + 8) * 1024);
    if (w < 2) dma16(src + (size_t)(w + 16) * 32 * NQ, lbase + WQ_L + (w + 16) * 1024);
  }
  const u32x2 ep = ld64(rs, EPOCH_OFF);
  const u32x4 xa = *(const u32x4*)(x + w * D + lane * 8);
  const u32x4 xb = *(const u32x4*)(x + w * D + 512 + (lane & 7) * 8);
  const size_t kvoff = ((size_t)(b * 16 + kvh) * SK + split * 2048 + w * 16) * 256;
  const char* kp = (const char*)kc + kvoff + (4 * (n & 3) + (n >> 2)) * 256 + a * 16;
  const char* vp = (const char*)vc + kvoff + lane * 16;
  u32x4 Kf[4], Vf[4];
#pragma unroll
  for (int s = 0; s < 4; ++s) Kf[s] = ldk(kp + 64 * s);
  MEMFENCE();
  ln_row(xa, xb, lane, L + XN_L + w * XN_STR);
  const u64 tag = (((u64)ep[1] << 32) | ep[0]) + 1ull;
  const unsigned tag_lo = (unsigned)tag, tag_hi = (unsigned)(tag >> 32);
  const unsigned gt_lo = (tag_lo & 0xffffu) << 16, gt_hi = (unsigned)(tag >> 16);
  WG_BARRIER();
  if (w == 0) {
    STAMP(1);
    f32x4 acc = {0.f, 0.f, 0.f, 0.f};
    const int aoff = WQ_L + (8 * a + (n >> 2)) * 32 + (n & 3) * 8;
    const int boff = XN_L + (n & 7) * XN_STR + a * 16;
#pragma unroll
    for (int s = 0; s < 18; ++s) acc = mfma16(tr_frag(L, aoff + s * 1024, aoff + s * 1024 + 128), lds128(L, boff + s * 64), acc);
    const unsigned pk = (n < 8) ? pkbf(acc[0], acc[1]) : pkbf(acc[2], acc[3]);
    st128(rs, QG_OFF + ((n & 7) * NQ + kvh * 256 + mem * 16 + 4 * a + 2 * (n >> 3)) * 8,
          u32x4{gt_lo | (pk & 0xffffu), gt_hi, gt_lo | (pk >> 16), gt_hi});
    MEMFENCE();
    STAMP(2);
    ldv4(Vf, vp, vnt);
    const int qoff = QG_OFF + (b * NQ + kvh * 256 + 4 * lane) * 8;
    u32x4 g0, g1;
    for (unsigned spins = 0;; ++spins) {
      MEMFENCE();
      g0 = ld128(rs, qoff); g1 = ld128(rs, qoff + 16);
      const bool ok = ((g0[0] ^ gt_lo) >> 16) == 0 && g0[1] == gt_hi && ((g0[2] ^ gt_lo) >> 16) == 0 && g0[3] == gt_hi &&
                      ((g1[0] ^ gt_lo) >> 16) == 0 && g1[1] == gt_hi && ((g1[2] ^ gt_lo) >> 16) == 0 && g1[3] == gt_hi;
      if (__all(ok) || spins > SPIN_CAP) break;
      __builtin_amdgcn_s_sleep(1);
    }
    *(LAS u32x2*)(L + Q_L + lane * 8) = u32x2{(g0[0] & 0xffffu) | (g0[2] << 16), (g1[0] & 0xffffu) | (g1[2] << 16)};
    STAMP(3);
  } else {
    const int w1 = w - 1;
    const u32x4 sent = {SENT, SENT, SENT, SENT};
    if (w1 == 0) { st128(rs, AR_OFF + bid * AR_STR + lane * 16, sent); if (lane == 0) st128(rs, AR_OFF + bid * AR_STR + 1024, sent); }
    if (is_wo) {
      const int c = w1 * 64 + lane;
      if (c < 288) st128(rs, HP_OFF + ((kvh * 8 + c / 36) * D + ns * 144) * 4 + (c % 36) * 16, sent);
      else if (c < 288 + 72) { const int e = c - 288; st128(rs, H_OFF + (e / 9) * H_REP + ((kvh >> 1) * D + ns * 144 + (kvh & 1) * 72) * 2 + (e % 9) * 16, sent); }
    } else {
#pragma unroll
      for (int t = 0; t < 3; ++t) { const int i = w1 + 7 * t; if (i < 18) st128(rs, FP_OFF + f * FP_STR + i * 1024 + lane * 16, sent); }
    }
    MEMFENCE();
#pragma unroll
    for (int t = 0; t < 12; ++t) {
      const int i = w1 + 7 * t;
      const int ci = i * 64 + lane;
      if (is_wo) {
        if (i < 72) { const int row = ci / 18, e = ci - row * 18; dma16(wo + (size_t)(kvh * 256 + row) * D + ns * 144 + e * 8, lbase + TW_L + i * 1024); }
      } else if (i < 54) {
        const int row = ci / 6, e = ci - row * 6;
        const u16* src = (e < 3 ? wg : wu) + (size_t)row * FF + f * 24 + (e < 3 ? e : e - 3) * 8;
        dma16(src, lbase + TW_L + i * 1024);
      } else if (i < 81) {
        dma16(wd + (size_t)f * 24 * D + (i - 54) * 512 + lane * 8, lbase + TW_L + i * 1024);
      }
      if ((t & 3) == 3) VMWAIT(4);
    }
    ldv4(Vf, vp, vnt);
    MEMFENCE();
  }
  WG_BARRIER();
  if (w == 1 && lane == 0) st64(rs, CF_OFF + bid * 8, u32x2{tag_lo, tag_hi});
  u32x4 Qf[4];
#pragma unroll
  for (int s = 0; s < 4; ++s) Qf[s] = lds128(L, Q_L + (n >> 3) * 256 + s * 64 + a * 16);
  u32x4 pmask;
#pragma unroll
  for (int i = 0; i < 4; ++i) pmask[i] = ((lane & 7) >> 1) == i ? ((lane & 1) ? 0xffff0000u : 0x0000ffffu) : 0u;
  const bool own_head = (lane >> 5) == ((lane >> 3) & 1);

  const float SCALE = 0.088388347648318440f, LOG2E = 1.4426950408889634f;
  float M = -INFINITY, ML = -INFINITY, lsum = 0.f;
  f32x4 O = {0.f, 0.f, 0.f, 0.f};
  auto step = [&](int it) {
    f32x4 S = {0.f, 0.f, 0.f, 0.f};
#pragma unroll
    for (int s = 0; s < 4; ++s) S = mfma16(Kf[s], Qf[s], S);
    if (it + 1 < 16) {
#pragma unroll
      for (int s = 0; s < 4; ++s) Kf[s] = ldk(kp + (size_t)(it + 1) * 32768 + 64 * s);
    }
    MEMFENCE();
    const unsigned r0 = pkbf(S[0], S[1]), r1 = pkbf(S[2], S[3]);
    const unsigned t0 = pkbf(bflo(r0) * SCALE, bfhi(r0) * SCALE), t1 = pkbf(bflo(r1) * SCALE, bfhi(r1) * SCALE);
    float sc[4] = {bflo(t0), bfhi(t0), bflo(t1), bfhi(t1)};
    float mx = fmaxf(fmaxf(sc[0], sc[1]), fmaxf(sc[2], sc[3]));
    if (__any(mx > M)) {
      mx = xrow_max(mx);
      const float Mn = fmaxf(M, mx), MLn = Mn * LOG2E;
      const float alpha = __builtin_amdgcn_exp2f(ML - MLn);
      const float al2 = dpp<ROR8>(alpha);
      const float ao = own_head ? alpha : al2;
      lsum *= alpha; O *= ao; M = Mn; ML = MLn;
    }
#pragma unroll
    for (int j = 0; j < 4; ++j) {
      const float p = __builtin_amdgcn_exp2f(fmaf(sc[j], LOG2E, -ML));
      lsum += p;
      const unsigned pk = pkbf(p, p);
      O = mfma16(u32x4{pk & pmask[0], pk & pmask[1], pk & pmask[2], pk & pmask[3]}, Vf[j], O);
    }
    if (it + 1 < 16) {
      ldv4(Vf, vp + (size_t)(it + 1) * 32768, vnt);
    }
    MEMFENCE();
  };
  step(0);
#pragma unroll 1
  for (int it = 1; it < 8; ++it) step(it);
  u32x4 c0 = {0, 0, 0, 0}, c1 = {0, 0, 0, 0};
  if (w == 7) { c0 = ld128(rs, CF_OFF + lane * 32); c1 = ld128(rs, CF_OFF + lane * 32 + 16); }
  MEMFENCE();
  step(8);
  if (w == 7) {
    auto cf_ok = [&](u32x4 p, u32x4 q) { return p[0] == tag_lo && p[1] == tag_hi && p[2] == tag_lo && p[3] == tag_hi &&
                                                q[0] == tag_lo && q[1] == tag_hi && q[2] == tag_lo && q[3] == tag_hi; };
    if (!__all(cf_ok(c0, c1))) {
      for (unsigned spins = 0;; ++spins) {
        __builtin_amdgcn_s_sleep(1);
        MEMFENCE();
        const u32x4 d0 = ld128(rs, CF_OFF + lane * 32), d1 = ld128(rs, CF_OFF + lane * 32 + 16);
        if (__all(cf_ok(d0, d1)) || spins > SPIN_CAP) break;
      }
    }
    if (bid == 0 && lane == 0) st64(rs, EPOCH_OFF, u32x2{tag_lo, tag_hi});
  }
#pragma unroll 1
  for (int it = 9; it < 15; ++it) step(it);
  step(15);
  if (w == 0) STAMP(4);
  lsum = xrow_sum(lsum);
  *(LAS f32x4*)(L + MG_L + w * MG_STR + ((a >> 1) * 128 + 8 * n + 4 * (a & 1)) * 4) = O;
  if ((lane & 55) == 0) *(LAS f32x2*)(L + MG_L + w * MG_STR + 1024 + (lane >> 3) * 8) = f32x2{ML, lsum};
  WG_BARRIER();
  if (w == 0) {
    STAMP(5);
    f32x2 ml[8]; f32x4 ow[8];
#pragma unroll
    for (int i = 0; i < 8; ++i) { ml[i] = *(LAS f32x2*)(L + MG_L + i * MG_STR + 1024 + (lane >> 5) * 8); ow[i] = *(LAS f32x4*)(L + MG_L + i * MG_STR + lane * 16); }
    float mxl = ml[0][0];
#pragma unroll
    for (int i = 1; i < 8; ++i) mxl = fmaxf(mxl, ml[i][0]);
    f32x4 o = {0.f, 0.f, 0.f, 0.f}; float ls = 0.f;
#pragma unroll
    for (int i = 0; i < 8; ++i) { const float wt = __builtin_amdgcn_exp2f(ml[i][0] - mxl); o += wt * ow[i]; ls = fmaf(wt, ml[i][1], ls); }
    st128(rs, AR_OFF + bid * AR_STR + lane * 16, canon4(o));
    if ((lane & 31) == 0) st64(rs, AR_OFF + bid * AR_STR + 1024 + (lane >> 5) * 8, u32x2{canon(mxl), canon(ls)});
    MEMFENCE();
    STAMP(6);
  }

  if (is_wo) {
    u32x4 A0[8], A1[8];
    const int aoff = TW_L + (8 * a + (n >> 2)) * 288 + (n & 3) * 8;
#pragma unroll
    for (int s = 0; s < 8; ++s) A0[s] = tr_frag(L, aoff + w * 32 + s * 32 * 288, aoff + w * 32 + s * 32 * 288 + 4 * 288);
    if (w == 7) {
#pragma unroll
      for (int s = 0; s < 8; ++s) A1[s] = tr_frag(L, aoff + 256 + s * 32 * 288, aoff + 256 + s * 32 * 288 + 4 * 288);
    }
    const int rb = kvh >> 1, rc = ns * 144 + (kvh & 1) * 72, ch = 4 * w + a;
    const bool rd = ch < 18;
    const u32x2 xr = *(const u32x2*)(x + rb * D + rc + (rd ? ch : 0) * 4);
    {
      const int r0 = AR_OFF + (xcd + 8 * ((jj & 16) + 2 * w)) * AR_STR, r1 = r0 + 8 * AR_STR;
      u32x4 o0, o1; u32x2 m0, m1;
      for (unsigned spins = 0;; ++spins) {
        MEMFENCE();
        o0 = ld128(rs, r0 + lane * 16); o1 = ld128(rs, r1 + lane * 16);
        m0 = ld64(rs, r0 + 1024 + (lane >> 5) * 8); m1 = ld64(rs, r1 + 1024 + (lane >> 5) * 8);
        if (__all(ok4(o0) && ok4(o1) && ok2(m0) && ok2(m1)) || spins > SPIN_CAP) break;
        __builtin_amdgcn_s_sleep(2);
      }
      const float ml0 = __uint_as_float(m0[0]), ml1 = __uint_as_float(m1[0]), mm = fmaxf(ml0, ml1);
      const float w0 = __builtin_amdgcn_exp2f(ml0 - mm), w1 = __builtin_amdgcn_exp2f(ml1 - mm);
      const float inv = 1.0f / (w0 * __uint_as_float(m0[1]) + w1 * __uint_as_float(m1[1]));
      const f32x4 o = (w0 * __builtin_bit_cast(f32x4, o0) + w1 * __builtin_bit_cast(f32x4, o1)) * inv;
      *(LAS u32x2*)(L + OB_L + w * OB_STR + lane * 8) = u32x2{pkbf(o[0], o[1]), pkbf(o[2], o[3])};
    }
    WG_BARRIER();
    if (w == 0) STAMP(7);
    {
      u32x4 Bf[8];
#pragma unroll
      for (int s = 0; s < 8; ++s) Bf[s] = lds128(L, OB_L + (n & 7) * OB_STR + s * 64 + a * 16);
      f32x4 acc = {0.f, 0.f, 0.f, 0.f};
#pragma unroll
      for (int s = 0; s < 8; ++s) acc = mfma16(A0[s], Bf[s], acc);
      const int hoff = HP_OFF + ((kvh * 8 + (n & 7)) * D + ns * 144 + 4 * a) * 4;
      if (w == 7) {
        f32x4 acc1 = {0.f, 0.f, 0.f, 0.f};
#pragma unroll
        for (int s = 0; s < 8; ++s) acc1 = mfma16(A1[s], Bf[s], acc1);
        st128(rs, hoff + (n < 8 ? 7 : 8) * 64, canon4(n < 8 ? acc : acc1));
      } else if (n < 8) st128(rs, hoff + w * 64, canon4(acc));
      MEMFENCE();
    }
    if (w == 0) STAMP(8);
    if (w < 5) {
      const int poff = HP_OFF + ((n * 8 + rb) * D + rc + (rd ? ch : 0) * 4) * 4;
      u32x4 pv;
      for (unsigned spins = 0;; ++spins) {
        MEMFENCE();
        pv = ld128(rs, poff);
        if (__all(ok4(pv)) || spins > SPIN_CAP) break;
        __builtin_amdgcn_s_sleep(1);
      }
      float s4[4];
#pragma unroll
      for (int i = 0; i < 4; ++i) s4[i] = row_sum(__uint_as_float(pv[i]));
      const unsigned a0 = pkbf(s4[0], s4[1]), a1 = pkbf(s4[2], s4[3]);
      const unsigned h0 = pkbf(bflo(xr[0]) + bflo(a0), bfhi(xr[0]) + bfhi(a0)), h1 = pkbf(bflo(xr[1]) + bflo(a1), bfhi(xr[1]) + bfhi(a1));
      if (rd && n < 8) st64(rs, H_OFF + n * H_REP + (rb * D + rc + ch * 4) * 2, u32x2{canonu(h0), canonu(h1)});
      if (w == 0) STAMP(9);
    }
    return;
  }

  u32x4 AG[18], AD[6];
  if (w < 3) {
    const int p = n & 3;
    const int aoff = TW_L + (8 * a + (n >> 2)) * 96 + (p >> 1) * 48 + w * 16 + (p & 1) * 8;
#pragma unroll
    for (int s = 0; s < 18; ++s) AG[s] = tr_frag(L, aoff + s * 32 * 96, aoff + s * 32 * 96 + 4 * 96);
  }
  {
    const int krow = (a < 3 ? 8 * a : 16) + (n >> 2);
    const int aoff = DN_L + krow * 1152 + (n & 3) * 8 + w * 64;
#pragma unroll
    for (int u = 0; u < 3; ++u) {
      if (u < 2 || w < 2) {
        AD[2 * u] = tr_frag(L, aoff + u * 512, aoff + u * 512 + 4 * 1152);
        AD[2 * u + 1] = tr_frag(L, aoff + u * 512 + 32, aoff + u * 512 + 32 + 4 * 1152);
      }
    }
  }
  if (w == 4 && lane < 8) *(LAS u32x4*)(L + ACT_L + lane * ACT_STR + 48) = u32x4{0, 0, 0, 0};
  {
    const int hoff = H_OFF + xcd * H_REP + w * D * 2;
    u32x4 ha, hb;
    for (unsigned spins = 0;; ++spins) {
      MEMFENCE();
      ha = ld128(rs, hoff + lane * 16); hb = ld128(rs, hoff + 1024 + (lane & 7) * 16);
      if (__all(ok4(ha) && ok4(hb)) || spins > SPIN_CAP) break;
      __builtin_amdgcn_s_sleep(4);
    }
    if (w == 0) STAMP(10);
    ln_row(ha, hb, lane, L + XN_L + w * XN_STR);
    *(LAS u32x4*)(L + HB_L + w * 1152 + lane * 16) = ha;
    if (lane < 8) *(LAS u32x4*)(L + HB_L + w * 1152 + 1024 + lane * 16) = hb;
  }
  WG_BARRIER();
  if (w == 0) STAMP(11);
  if (w < 3) {
    f32x4 acc = {0.f, 0.f, 0.f, 0.f};
    const int boff = XN_L + (n & 7) * XN_STR + a * 16;
#pragma unroll
    for (int s = 0; s < 18; ++s) acc = mfma16(AG[s], lds128(L, boff + s * 64), acc);
    const int j = 2 * (lane >> 5) + (n >> 3);
    const float keep = j == 0 ? acc[0] : j == 1 ? acc[1] : j == 2 ? acc[2] : acc[3];
    const int jo = j ^ 2;
    const float give = jo == 0 ? acc[0] : jo == 1 ? acc[1] : jo == 2 ? acc[2] : acc[3];
    auto sw = __builtin_amdgcn_permlane32_swap(__float_as_uint(give), __float_as_uint(give), false, false);
    const float got = __uint_as_float(lane < 32 ? sw[1] : sw[0]);
    const float g = rbf(lane < 32 ? keep : got), uu = rbf(lane < 32 ? got : keep);
    const float z = 0.7978845608028654f * (g + 0.044715f * g * g * g);
    const float ge = g * __builtin_amdgcn_rcpf(1.0f + __builtin_amdgcn_exp2f(-2.0f * 1.4426950408889634f * z));
    const float act = rbf(ge) * uu;
    *(LAS u16*)(L + ACT_L + (n & 7) * ACT_STR + (8 * w + 4 * (a & 1) + j) * 2) = (u16)(pkbf(act, act) & 0xffffu);
  }
  WG_BARRIER();
  if (w == 0) STAMP(12);
  {
    const u32x4 Bf = lds128(L, ACT_L + (n & 7) * ACT_STR + a * 16);
    const f32x4 z4 = {0.f, 0.f, 0.f, 0.f};
#pragma unroll
    for (int u = 0; u < 3; ++u) {
      if (u < 2 || w < 2) {
        const f32x4 d0 = mfma16(AD[2 * u], Bf, z4), d1 = mfma16(AD[2 * u + 1], Bf, z4);
        st128(rs, FP_OFF + f * FP_STR + ((n & 7) * D + (w + 8 * u) * 32 + (n >> 3) * 16 + 4 * a) * 4, canon4(n < 8 ? d0 : d1));
      }
    }
    MEMFENCE();
  }
  if (w == 0) STAMP(13);
  if ((f & 3) == 3) return;
  {
    const int r = (f >> 2) * 3 + (f & 3), rb = r / 18, c0 = (r - rb * 18) * 32;
    const int poff = FP_OFF + (tid >> 3) * FP_STR + (rb * D + c0 + (lane & 7) * 4) * 4;
    u32x4 p0, p1, p2;
    for (unsigned spins = 0;; ++spins) {
      MEMFENCE();
      p0 = ld128(rs, poff); p1 = ld128(rs, poff + 64 * FP_STR); p2 = ld128(rs, poff + 128 * FP_STR);
      if (__all(ok4(p0) && ok4(p1) && ok4(p2)) || spins > SPIN_CAP) break;
      __builtin_amdgcn_s_sleep(1);
    }
    if (w == 0) STAMP(14);
    f32x4 s = (__builtin_bit_cast(f32x4, p0) + __builtin_bit_cast(f32x4, p1)) + __builtin_bit_cast(f32x4, p2);
#pragma unroll
    for (int i = 0; i < 4; ++i) { float t = s[i]; t += dpp<ROR8>(t); s[i] = xrow_sum(t); }
    if (lane < 8) *(LAS f32x4*)(L + R2_L + w * 128 + lane * 16) = s;
    WG_BARRIER();
    if (w == 0 && lane < 8) {
      f32x4 t = {0.f, 0.f, 0.f, 0.f};
#pragma unroll
      for (int i = 0; i < 8; ++i) t += *(LAS f32x4*)(L + R2_L + i * 128 + lane * 16);
      const u32x2 hv = *(LAS u32x2*)(L + HB_L + rb * 1152 + (c0 + lane * 4) * 2);
      const unsigned y0 = pkbf(t[0], t[1]), y1 = pkbf(t[2], t[3]);
      *(u32x2*)(out + rb * D + c0 + lane * 4) =
          u32x2{pkbf(bflo(hv[0]) + bflo(y0), bfhi(hv[0]) + bfhi(y0)), pkbf(bflo(hv[1]) + bflo(y1), bfhi(hv[1]) + bfhi(y1))};
    }
    if (w == 0) STAMP(15);
  }
}

extern "C" void kernel_launch(void* const* d_in, const int* in_sizes, int n_in,
                              void* d_out, int out_size, void* d_ws, size_t ws_size,
                              hipStream_t stream) {
  fused_block<<<NWG, NTHR, 0, stream>>>((const u16*)d_in[0], (const u16*)d_in[1], (const u16*)d_in[2], (const u16*)d_in[3],
                                        (const u16*)d_in[4], (const u16*)d_in[6], (const u16*)d_in[7], (const u16*)d_in[8],
                                        (u16*)d_out, (unsigned char*)d_ws);
}
```

```cpp
#include <hip/hip_runtime.h>
#include <stdint.h>

#define LAS __attribute__((address_space(3)))
typedef unsigned short u16;
typedef unsigned long long u64;
typedef unsigned u32x4 __attribute__((ext_vector_type(4)));
typedef unsigned u32x2 __attribute__((ext_vector_type(2)));
typedef float f32x4 __attribute__((ext_vector_type(4)));
typedef float f32x2 __attribute__((ext_vector_type(2)));
typedef short s16x8 __attribute__((ext_vector_type(8)));
typedef short s16x4 __attribute__((ext_vector_type(4)));
typedef __bf16 bf16x2 __attribute__((ext_vector_type(2)));

#define HPACE 3
constexpr int NPL = 2;
constexpr int PACE = 115;
constexpr int POFF = 100;
constexpr int THINK = 6;
constexpr int QDELAY = 520;
constexpr int CFT = 2600;
constexpr int CFR = 2;

constexpr int D = 576, SK = 4096, FF = 4608, NQ = 4096;
constexpr int NWG = 256, NTHR = 576, NFFN = 144;

constexpr int EPOCH_OFF = 0;
constexpr int CF_OFF = 4096;
constexpr int QG_OFF = CF_OFF + 16 * 2048;
constexpr int AR_OFF = QG_OFF + 8 * 16 * 8192;
constexpr int AR_STR = 1152;
constexpr int HP_OFF = AR_OFF + NWG * AR_STR;
constexpr int H_OFF = HP_OFF + 16 * 8 * D * 4;
constexpr int H_REP = 8 * D * 2;
constexpr int AC_OFF = H_OFF + 8 * H_REP;
constexpr int FP_OFF = AC_OFF + NFFN * 512;
constexpr int GD_OFF = FP_OFF + 16 * 8 * D * 4;
constexpr int WS_BYTES = GD_OFF + 8 * 128;
constexpr unsigned SENT = 0xFFFFFFFFu;

constexpr int WQ_L = 0;
constexpr int DN_L = 18432;
constexpr int GU_L = DN_L + 36864;
constexpr int KP_L = GU_L + 12288;
constexpr int XN_L = GU_L + 77824;
constexpr int XN_STR = 1168;
constexpr int Q_L = XN_L + 8 * XN_STR;
constexpr int MG_L = Q_L + 512;
constexpr int MG_STR = 1040;
constexpr int CNT_L = MG_L + 8 * MG_STR;
constexpr int LDS_BYTES = CNT_L + 16;
constexpr int OB_L = XN_L;
constexpr int OB_STR = 528;
constexpr unsigned QREADY = 1u << 30;
static_assert(LDS_BYTES <= 160 * 1024, "LDS");

constexpr unsigned SPIN_CAP = 1u << 18;

__device__ __forceinline__ float bflo(unsigned u) { return __uint_as_float(u << 16); }
__device__ __forceinline__ float bfhi(unsigned u) { return __uint_as_float(u & 0xffff0000u); }
__device__ __forceinline__ unsigned pkbf(float lo, float hi) { return __builtin_bit_cast(unsigned, __builtin_convertvector(f32x2{lo, hi}, bf16x2)); }
__device__ __forceinline__ float rbf(float x) { return bflo(pkbf(x, x)); }
__device__ __forceinline__ f32x4 mfma16(u32x4 a, u32x4 b, f32x4 c) {
  return __builtin_amdgcn_mfma_f32_16x16x32_bf16(__builtin_bit_cast(s16x8, a), __builtin_bit_cast(s16x8, b), c, 0, 0, 0);
}
template <int CTRL> __device__ __forceinline__ float dpp(float x) {
  return __builtin_bit_cast(float, __builtin_amdgcn_mov_dpp(__builtin_bit_cast(int, x), CTRL, 0xf, 0xf, true));
}
constexpr int XOR1 = 0xB1, XOR2 = 0x4E, XOR7 = 0x141, ROR8 = 0x128;
__device__ __forceinline__ float xrow_max(float x) {
  auto s = __builtin_amdgcn_permlane16_swap(__float_as_uint(x), __float_as_uint(x), false, false);
  x = fmaxf(__uint_as_float(s[0]), __uint_as_float(s[1]));
  auto t = __builtin_amdgcn_permlane32_swap(__float_as_uint(x), __float_as_uint(x), false, false);
  return fmaxf(__uint_as_float(t[0]), __uint_as_float(t[1]));
}
__device__ __forceinline__ float xrow_sum(float x) {
  auto s = __builtin_amdgcn_permlane16_swap(__float_as_uint(x), __float_as_uint(x), false, false);
  x = __uint_as_float(s[0]) + __uint_as_float(s[1]);
  auto t = __builtin_amdgcn_permlane32_swap(__float_as_uint(x), __float_as_uint(x), false, false);
  return __uint_as_float(t[0]) + __uint_as_float(t[1]);
}
__device__ __forceinline__ float row_sum(float x) {
  x += dpp<XOR1>(x); x += dpp<XOR2>(x); x += dpp<XOR7>(x); x += dpp<ROR8>(x); return x;
}
__device__ __forceinline__ float wave_sum(float x) { return xrow_sum(row_sum(x)); }
__device__ __forceinline__ unsigned clk32() { return (unsigned)__builtin_amdgcn_s_memrealtime(); }

#define WG_BARRIER() do { asm volatile("s_waitcnt lgkmcnt(0)" ::: "memory"); __builtin_amdgcn_s_barrier(); asm volatile("" ::: "memory"); } while (0)
#define VMWAIT_(n) asm volatile("s_waitcnt vmcnt(" #n ")" ::: "memory")
#define VMWAIT(n) VMWAIT_(n)
#define MEMFENCE() asm volatile("" ::: "memory")
#define PIN(v) asm volatile("" : "+v"(v) :: "memory")
#define PIN4(R) asm volatile("" : "+v"(R[0]), "+v"(R[1]), "+v"(R[2]), "+v"(R[3]) :: "memory")

#define DMA_ASM(INSN) \
  unsigned keep; \
  asm volatile("s_mov_b32 %0, m0\n\ts_mov_b32 m0, %2\n\ts_nop 0\n\t" INSN "\n\ts_mov_b32 m0, %0" : "=&s"(keep) : "v"(gsrc), "s"(lds_dst) : "memory")
__device__ __forceinline__ void dma16(const void* gsrc, unsigned lds_dst) { DMA_ASM("global_load_lds_dwordx4 %1, off"); }
__device__ __forceinline__ void dma16nt(const void* gsrc, unsigned lds_dst) { DMA_ASM("global_load_lds_dwordx4 %1, off nt"); }
__device__ __forceinline__ void dma4(const void* gsrc, unsigned lds_dst) { DMA_ASM("global_load_lds_dword %1, off"); }

typedef __amdgpu_buffer_rsrc_t rsrc_t;
__device__ __forceinline__ u32x4 ld128(rsrc_t rs, int off) { return __builtin_amdgcn_raw_buffer_load_b128(rs, off, 0, 16); }
__device__ __forceinline__ u32x2 ld64(rsrc_t rs, int off) { return __builtin_amdgcn_raw_buffer_load_b64(rs, off, 0, 16); }
__device__ __forceinline__ unsigned ld32(rsrc_t rs, int off) { return __builtin_amdgcn_raw_buffer_load_b32(rs, off, 0, 16); }
__device__ __forceinline__ void st128(rsrc_t rs, int off, u32x4 v) { __builtin_amdgcn_raw_buffer_store_b128(v, rs, off, 0, 16); }
__device__ __forceinline__ void st64(rsrc_t rs, int off, u32x2 v) { __builtin_amdgcn_raw_buffer_store_b64(v, rs, off, 0, 16); }
__device__ __forceinline__ void st32(rsrc_t rs, int off, unsigned v) { __builtin_amdgcn_raw_buffer_store_b32(v, rs, off, 0, 16); }
__device__ __forceinline__ unsigned canonu(unsigned u) { return u < 0xFFFFFFFEu ? u : 0xFFFFFFFEu; }
__device__ __forceinline__ unsigned canon(float v) { return canonu(__float_as_uint(v)); }
__device__ __forceinline__ u32x4 canon4(f32x4 v) { return u32x4{canon(v[0]), canon(v[1]), canon(v[2]), canon(v[3])}; }
__device__ __forceinline__ unsigned mxu(unsigned a, unsigned b) { return a > b ? a : b; }
__device__ __forceinline__ unsigned mx4(u32x4 v) { return mxu(mxu(v[0], v[1]), mxu(v[2], v[3])); }
__device__ __forceinline__ unsigned mx2(u32x2 v) { return mxu(v[0], v[1]); }
__device__ __forceinline__ unsigned lds_add(LAS unsigned char* L, int off, unsigned v, int lane) {
  unsigned r = 0;
  if (lane == 0) r = __hip_atomic_fetch_add((LAS unsigned*)(L + off), v, __ATOMIC_RELAXED, __HIP_MEMORY_SCOPE_WORKGROUP);
  return __builtin_amdgcn_readfirstlane(r);
}

__device__ __forceinline__ u32x4 lds128(LAS unsigned char* L, int off) { return *(LAS u32x4*)(L + off); }
__device__ __forceinline__ u32x4 tr_frag(LAS unsigned char* L, int off, int off2) {
  const s16x4 a = __builtin_amdgcn_ds_read_tr16_b64_v4i16((LAS s16x4*)(L + off));
  const s16x4 b = __builtin_amdgcn_ds_read_tr16_b64_v4i16((LAS s16x4*)(L + off2));
  const u32x2 x = __builtin_bit_cast(u32x2, a), y = __builtin_bit_cast(u32x2, b);
  return u32x4{x[0], x[1], y[0], y[1]};
}

__device__ __forceinline__ void ln_row(u32x4 xa, unsigned xb, int lane, LAS unsigned char* dst) {
  float v[10];
#pragma unroll
  for (int i = 0; i < 4; ++i) { v[2 * i] = bflo(xa[i]); v[2 * i + 1] = bfhi(xa[i]); }
  v[8] = bflo(xb); v[9] = bfhi(xb);
  const bool tail = lane < 32;
  const float x0 = __uint_as_float(__builtin_amdgcn_readfirstlane(xa[0] << 16));
  float d[10];
#pragma unroll
  for (int i = 0; i < 10; ++i) d[i] = v[i] - x0;
  if (!tail) { d[8] = 0.f; d[9] = 0.f; }
  float s1 = (((d[0] + d[1]) + (d[2] + d[3])) + ((d[4] + d[5]) + (d[6] + d[7]))) + (d[8] + d[9]);
  float qa = d[0] * d[0], qb = d[1] * d[1];
#pragma unroll
  for (int i = 2; i < 10; i += 2) { qa = fmaf(d[i], d[i], qa); qb = fmaf(d[i + 1], d[i + 1], qb); }
  float s2 = qa + qb;
  s1 = wave_sum(s1); s2 = wave_sum(s2);
  const float md = s1 * (1.f / D);
  const float var = fmaxf(fmaf(-md, md, s2 * (1.f / D)), 0.f);
  const float mb = rbf(x0 + md);
  const float sd = rbf(__builtin_amdgcn_sqrtf(rbf(rbf(var) + 1e-5f)));
  const float r = __builtin_amdgcn_rcpf(sd);
  unsigned y[5];
#pragma unroll
  for (int i = 0; i < 5; ++i) { const unsigned c = pkbf(v[2 * i] - mb, v[2 * i + 1] - mb); y[i] = pkbf(bflo(c) * r, bfhi(c) * r); }
  *(LAS u32x4*)(dst + lane * 16) = u32x4{y[0], y[1], y[2], y[3]};
  if (tail) *(LAS unsigned*)(dst + 1024 + lane * 4) = y[4];
}

struct Tail {
  bool is_ffn, is_wo, is_red;
  int f, wkv, ns, kg, nsl, rb, rc;
};
__device__ __forceinline__ Tail tail_roles(int bid) {
  asm volatile("" : "+s"(bid));
  const int xcd = bid & 7, jj = bid >> 3;
  Tail t;
  t.is_ffn = jj < 18; t.is_wo = jj >= 18 && jj < 30;
  t.f = xcd * 18 + jj;
  t.kg = 2 * xcd + (jj >= 9 ? 1 : 0); t.nsl = jj >= 9 ? jj - 9 : jj;
  t.wkv = 2 * xcd + (jj >= 24 ? 1 : 0); t.ns = jj >= 24 ? jj - 24 : jj - 18;
  const int u = xcd * 12 + (jj - 18), rr = (u >> 2) * 3 + (u & 3);
  t.is_red = t.is_wo && (u & 3) != 3;
  t.rb = rr / 9; t.rc = (rr - t.rb * 9) * 64;
  return t;
}

__global__ void __launch_bounds__(NTHR, 3)
fused_block(const u16* __restrict__ x, const u16* __restrict__ kc, const u16* __restrict__ vc, const u16* __restrict__ wq,
            const u16* __restrict__ wo, const u16* __restrict__ wg, const u16* __restrict__ wu, const u16* __restrict__ wd,
            u16* __restrict__ out, unsigned char* __restrict__ ws) {
  __shared__ __attribute__((aligned(16))) unsigned char lds_raw[LDS_BYTES];
  LAS unsigned char* L = (LAS unsigned char*)lds_raw;
  const unsigned lbase = (unsigned)(size_t)L;
  const int tid = threadIdx.x, lane = tid & 63, w = __builtin_amdgcn_readfirstlane(tid >> 6);
  const int bid = blockIdx.x, xcd = bid & 7, jj = bid >> 3;
  const int kvh = 2 * xcd + (jj >> 4), mem = jj & 15, ab = mem >> 1, split = mem & 1;
  const int a = lane >> 4, n = lane & 15;
  asm volatile("" :: "s"(x), "s"(kc), "s"(vc), "s"(wq), "s"(wo), "s"(wg), "s"(wu), "s"(wd), "s"(out), "s"(ws));
  const rsrc_t rs = __builtin_amdgcn_make_buffer_rsrc((void*)ws, 0, WS_BYTES, 0x00020000);
  auto wo_job = [&](const Tail& t, int i) {
    const unsigned ci = i * 64 + lane, row = (ci * 87382u) >> 20, e = ci - row * 12u;
    dma16((const char*)(wo + (size_t)t.wkv * 256 * D + t.ns * 96) + (row * 1152u + e * 16u), lbase + DN_L + i * 1024);
  };

  if (w == 8) {
    __builtin_amdgcn_s_setprio(3);
    const int cb = mem & 3, kq = mem >> 2;
#pragma unroll 1
    for (int i = 0; i < 18; ++i) {
      const int row = i * 8 + (lane >> 3), c = (lane & 7) ^ ((row & 2) | ((row >> 1) & 4));
      dma16((const char*)wq + ((kq * 144 + row) * (NQ * 2) + kvh * 512 + cb * 128 + c * 16), lbase + WQ_L + i * 1024);
    }
    const u32x2 ep = ld64(rs, EPOCH_OFF);
    const unsigned tstart = clk32();
    if (lane == 0) *(LAS u32x4*)(L + CNT_L) = u32x4{0u, 0u, 0u, 0u};
    WG_BARRIER();
    const u64 tag = (((u64)ep[1] << 32) | ep[0]) + 1ull;
    const unsigned tag_lo = (unsigned)tag, tag_hi = (unsigned)(tag >> 32);
    const unsigned tg0 = tag_lo, tg1 = tag_lo * 0x9E3779B1u + tag_hi;
    VMWAIT(0);
    u32x4 A[4][5];
    {
      const int p = n & 3, q = n >> 2, sw = (q & 2) | ((a & 1) << 2);
#pragma unroll
      for (int T = 0; T < 4; ++T) {
        const int cofs = ((2 * T + (p >> 1)) ^ sw) * 16 + (p & 1) * 8;
#pragma unroll
        for (int s = 0; s < 5; ++s) {
          const int o = WQ_L + (32 * s + 8 * (s < 4 ? a : (a & 1)) + q) * 128 + cofs;
          A[T][s] = tr_frag(L, o, o + 512);
        }
      }
    }
#pragma unroll 1
    for (unsigned spins = 0; spins < (SPIN_CAP << 4); ++spins) {
      if (__builtin_amdgcn_readfirstlane(*(volatile LAS unsigned*)(L + CNT_L + 8)) == 8u) break;
    }
    MEMFENCE();
    {
      u32x4 Bq[5];
#pragma unroll
      for (int s = 0; s < 5; ++s) Bq[s] = lds128(L, XN_L + (n & 7) * XN_STR + kq * 288 + s * 64 + (s < 4 ? a : (a & 1)) * 16);
      if (a >= 2) Bq[4] = u32x4{0u, 0u, 0u, 0u};
      f32x4 c[4];
#pragma unroll
      for (int T = 0; T < 4; ++T) c[T] = f32x4{0.f, 0.f, 0.f, 0.f};
#pragma unroll
      for (int s = 0; s < 5; ++s)
#pragma unroll
        for (int T = 0; T < 4; ++T) c[T] = mfma16(A[T][s], Bq[s], c[T]);
      const int so = QG_OFF + ((n & 7) * 16 + kvh) * 8192 + kq * 2048 + (cb * 64 + 4 * a + 2 * (n >> 3)) * 8;
#pragma unroll
      for (int T = 0; T < 4; ++T)
        st128(rs, so + T * 128, u32x4{__float_as_uint(n < 8 ? c[T][0] : c[T][2]), tg0, __float_as_uint(n < 8 ? c[T][1] : c[T][3]), tg1});
    }
    MEMFENCE();
    const Tail t = tail_roles(bid);
    const int arec = kvh * 16 + mem;
    {
      const u32x4 sent = {SENT, SENT, SENT, SENT};
      st128(rs, AR_OFF + arec * AR_STR + lane * 16, sent);
      if (lane == 0) st128(rs, AR_OFF + arec * AR_STR + 1024, sent);
      if (t.is_wo) {
#pragma unroll 1
        for (int c = lane; c < 192; c += 64) st128(rs, HP_OFF + ((t.wkv * 8 + c / 24) * D + t.ns * 96) * 4 + (c % 24) * 16, sent);
        if (t.ns == 0 && lane < 8) st32(rs, GD_OFF + lane * 128 + t.wkv * 4, SENT);
        if (t.is_red) st128(rs, H_OFF + (lane >> 3) * H_REP + (t.rb * D + t.rc) * 2 + (lane & 7) * 16, sent);
      } else if (t.is_ffn) {
        if (lane < 32) st128(rs, AC_OFF + t.f * 512 + lane * 16, sent);
        st128(rs, FP_OFF + ((t.kg * 8 + (lane >> 4)) * D + t.nsl * 64) * 4 + (lane & 15) * 16, sent);
        st128(rs, FP_OFF + ((t.kg * 8 + 4 + (lane >> 4)) * D + t.nsl * 64) * 4 + (lane & 15) * 16, sent);
      }
      MEMFENCE();
    }
    {
      const int qoff = QG_OFF + (ab * 16 + kvh) * 8192 + lane * 32;
      u32x4 g[8];
      while ((int)(clk32() - tstart) < QDELAY) __builtin_amdgcn_s_sleep(2);
#pragma unroll
      for (int i = 0; i < 8; ++i) g[i] = ld128(rs, qoff + (i >> 1) * 2048 + (i & 1) * 16);
#pragma unroll 1
      for (unsigned spins = 0; spins <= SPIN_CAP; ++spins) {
        bool bad[4];
#pragma unroll
        for (int k = 0; k < 4; ++k) bad[k] = ((g[2 * k][1] ^ tg0) | (g[2 * k][3] ^ tg1) | (g[2 * k + 1][1] ^ tg0) | (g[2 * k + 1][3] ^ tg1)) != 0u;
        if (!__any(bad[0] || bad[1] || bad[2] || bad[3])) break;
        __builtin_amdgcn_s_sleep(16);
        MEMFENCE();
#pragma unroll
        for (int k = 0; k < 4; ++k) if (bad[k]) { g[2 * k] = ld128(rs, qoff + k * 2048); g[2 * k + 1] = ld128(rs, qoff + k * 2048 + 16); }
      }
      float qv[4];
#pragma unroll
      for (int e = 0; e < 4; ++e) {
        const int h = e >> 1, d = (e & 1) * 2;
        qv[e] = (__uint_as_float(g[h][d]) + __uint_as_float(g[2 + h][d])) + (__uint_as_float(g[4 + h][d]) + __uint_as_float(g[6 + h][d]));
      }
      *(LAS u32x2*)(L + Q_L + lane * 8) = u32x2{pkbf(qv[0], qv[1]), pkbf(qv[2], qv[3])};
    }
    asm volatile("s_waitcnt lgkmcnt(0)" ::: "memory");
    lds_add(L, CNT_L + 4, QREADY, lane);
    VMWAIT(0);
    if (lane < CFR) st64(rs, CF_OFF + lane * 2048 + bid * 8, u32x2{tag_lo, tag_hi});
    MEMFENCE();
    if (t.is_wo) {
      for (;;) {
        const int i = (int)(lds_add(L, CNT_L + 4, 1u, lane) & (QREADY - 1));
        if (i >= 48) break;
        wo_job(t, i);
        VMWAIT(HPACE);
      }
    }
    while ((int)(clk32() - tstart) < CFT) __builtin_amdgcn_s_sleep(32);
    for (unsigned spins = 0;; ++spins) {
      MEMFENCE();
      const u32x4 d0 = ld128(rs, CF_OFF + (bid & (CFR - 1)) * 2048 + lane * 32), d1 = ld128(rs, CF_OFF + (bid & (CFR - 1)) * 2048 + lane * 32 + 16);
      const unsigned bad = (d0[0] ^ tag_lo) | (d0[1] ^ tag_hi) | (d0[2] ^ tag_lo) | (d0[3] ^ tag_hi) |
                           (d1[0] ^ tag_lo) | (d1[1] ^ tag_hi) | (d1[2] ^ tag_lo) | (d1[3] ^ tag_hi);
      if (__all(bad == 0) || spins > SPIN_CAP) break;
      __builtin_amdgcn_s_sleep(64);
    }
    if (bid == 0 && lane == 0) st64(rs, EPOCH_OFF, u32x2{tag_lo, tag_hi});
    VMWAIT(0);
#pragma unroll 1
    for (unsigned spins = 0; spins < (SPIN_CAP << 6); ++spins) {
      if (__builtin_amdgcn_readfirstlane(*(volatile LAS unsigned*)(L + CNT_L + 12)) >= 8u) break;
      __builtin_amdgcn_s_sleep(2);
    }
#pragma unroll 1
    for (int k = 0; k < 324; k += 64) {
      const int e0 = k + lane, e = e0 < 324 ? e0 : 323;
      const int i = jj * 324 + e, m = i >= 5184 ? 1 : 0, r9 = i - m * 5184, r = r9 / 9, c = r9 - r * 9;
      dma4((const char*)(m ? wu : wg) + (r * (FF * 2) + xcd * 1152 + c * 128), lbase + WQ_L);
    }
    WG_BARRIER();
    f32x2 ml[8]; f32x4 ow[8];
#pragma unroll
    for (int i = 0; i < 8; ++i) { ml[i] = *(LAS f32x2*)(L + MG_L + i * MG_STR + 1024 + (lane >> 5) * 8); ow[i] = *(LAS f32x4*)(L + MG_L + i * MG_STR + lane * 16); }
    __builtin_amdgcn_sched_barrier(0);
    float mxl = ml[0][0];
#pragma unroll
    for (int i = 1; i < 8; ++i) mxl = fmaxf(mxl, ml[i][0]);
    f32x4 o = {0.f, 0.f, 0.f, 0.f}; float ls = 0.f;
#pragma unroll
    for (int i = 0; i < 8; ++i) { const float wgt = __builtin_amdgcn_exp2f(ml[i][0] - mxl); o += wgt * ow[i]; ls = fmaf(wgt, ml[i][1], ls); }
    st128(rs, AR_OFF + arec * AR_STR + lane * 16, canon4(o));
    {
      auto sm = __builtin_amdgcn_permlane32_swap(__float_as_uint(mxl), __float_as_uint(mxl), false, false);
      auto sl = __builtin_amdgcn_permlane32_swap(__float_as_uint(ls), __float_as_uint(ls), false, false);
      if (lane == 0) st128(rs, AR_OFF + arec * AR_STR + 1024, u32x4{canonu(sm[0]), canonu(sl[0]), canonu(sm[1]), canonu(sl[1])});
    }
    MEMFENCE();
    if (t.is_wo || t.is_ffn) WG_BARRIER();
    return;
  }

  {
    const size_t kvoff = ((size_t)(ab * 16 + kvh) * SK + split * 2048) * 128;
    const rsrc_t rk = __builtin_amdgcn_make_buffer_rsrc((void*)(kc + kvoff), 0, 2048 * 256, 0x00020000);
    const int koff = (4 * (n & 3) + ((n >> 2) & 1)) * 256 + (n >> 3) * 64 + a * 16, voff = lane * 16;
    auto ldk = [&](u32x4 (&R)[4], int t) {
#pragma unroll
      for (int u = 0; u < 4; ++u) R[u] = __builtin_amdgcn_raw_buffer_load_b128(rk, koff + (u & 1) * 512 + (u >> 1) * 128, t * 4096, 2);
      MEMFENCE();
    };
    u32x4 K0[4], K1[4], K2[4], K3[4], K4[4], K5[4];
    const u32x4 xa = *(const u32x4*)(x + w * D + lane * 8);
    const unsigned xb = *(const unsigned*)(x + w * D + 512 + (lane & 31) * 2);
    MEMFENCE();
    ldk(K0, w);
    unsigned due = clk32() + (POFF + (6 + NPL) * PACE);
    ldk(K1, w + 8);
    WG_BARRIER();
    ln_row(xa, xb, lane, L + XN_L + w * XN_STR);
    asm volatile("s_waitcnt lgkmcnt(0)" ::: "memory");
    lds_add(L, CNT_L + 8, 1u, lane);
    VMWAIT(0); ldk(K2, w + 16);
    VMWAIT(0); ldk(K3, w + 24);
    VMWAIT(0); ldk(K4, w + 32);
    VMWAIT(0); ldk(K5, w + 40);
#pragma unroll 1
    for (int i = 0; i < NPL; ++i) {
      const char* src = (const char*)(kc + kvoff) + ((w + 48 + 8 * i) * 4096 + koff);
      VMWAIT(0);
#pragma unroll
      for (int u = 0; u < 4; ++u) dma16nt(src + ((u & 1) * 512 + (u >> 1) * 128), lbase + KP_L + (w * 2 + i) * 4096 + u * 1024);
    }
    {
      const Tail t = tail_roles(bid);
#pragma unroll 1
      for (unsigned spins = 0; spins < (SPIN_CAP << 4); ++spins) {
        if (__builtin_amdgcn_readfirstlane(*(volatile LAS unsigned*)(L + CNT_L + 4)) & QREADY) break;
        VMWAIT(2);
        int i = 48;
        if (t.is_wo) i = (int)(lds_add(L, CNT_L + 4, 1u, lane) & (QREADY - 1));
        if (i < 48) wo_job(t, i); else __builtin_amdgcn_s_sleep(4);
      }
    }
    MEMFENCE();
    u32x4 Qf[4];
#pragma unroll
    for (int s = 0; s < 4; ++s) Qf[s] = lds128(L, Q_L + (n >> 3) * 256 + s * 64 + a * 16);
    const float SCALE = 0.088388347648318440f, LOG2E = 1.4426950408889634f;
    const int scoff = MG_L + w * MG_STR + a * 16 + (n >> 3) * 8;
    float M = -INFINITY;
    auto qk = [&](u32x4 (&K)[4]) {
      u32x4 Kf[4];
#pragma unroll
      for (int h = 0; h < 2; ++h)
#pragma unroll
        for (int i = 0; i < 4; ++i) {
          Kf[2 * h][i] = __builtin_amdgcn_update_dpp(K[2 * h][i], K[2 * h + 1][i], ROR8, 0xf, 0xc, false);
          Kf[2 * h + 1][i] = __builtin_amdgcn_update_dpp(K[2 * h + 1][i], K[2 * h][i], ROR8, 0xf, 0x3, false);
        }
      f32x4 S = {0.f, 0.f, 0.f, 0.f};
#pragma unroll
      for (int s = 0; s < 4; ++s) S = mfma16(Kf[s], Qf[s], S);
      PIN(S);
      return S;
    };
    auto keep = [&](f32x4 S, int it) {
      const unsigned r0 = pkbf(S[0], S[1]), r1 = pkbf(S[2], S[3]);
      const unsigned t0 = pkbf(bflo(r0) * SCALE, bfhi(r0) * SCALE), t1 = pkbf(bflo(r1) * SCALE, bfhi(r1) * SCALE);
      M = fmaxf(fmaxf(M, fmaxf(bflo(t0), bfhi(t0))), fmaxf(bflo(t1), bfhi(t1)));
      if ((n & 7) == 0) *(LAS u32x2*)(L + scoff + it * 64) = u32x2{t0, t1};
    };
    auto gate = [&](unsigned now) {
      __builtin_amdgcn_s_sleep(THINK);
      due += PACE;
      while ((int)(now - due) < 0) now = clk32();
    };
    constexpr int NB = 6 + NPL;
    { const f32x4 S = qk(K0); VMWAIT(0); ldk(K0, w + 8 * NB); keep(S, 0); }
    keep(qk(K1), 1); keep(qk(K2), 2); keep(qk(K3), 3); keep(qk(K4), 4);
    VMWAIT(0); ldk(K1, w + 8 * NB + 8);
    keep(qk(K5), 5);
#pragma unroll 1
    for (int i = 0; i < NPL; ++i) {
#pragma unroll
      for (int u = 0; u < 4; ++u) K2[u] = lds128(L, KP_L + (w * 2 + i) * 4096 + u * 1024 + lane * 16);
      keep(qk(K2), 6 + i);
    }
    { const f32x4 S = qk(K0); VMWAIT(0); due += 2 * PACE; ldk(K0, w + 8 * NB + 16); keep(S, NB); }
    keep(qk(K1), NB + 1);
#pragma unroll 1
    for (int it = NB + 2; it < 15; ++it) {
      const unsigned now = clk32();
      const f32x4 S = qk(K0);
      gate(now);
      ldk(K0, w + 8 * it + 8);
      keep(S, it);
    }
    const rsrc_t rv = __builtin_amdgcn_make_buffer_rsrc((void*)(vc + kvoff), 0, 2048 * 256, 0x00020000);
    auto ldv = [&](u32x4 (&R)[4], int t) {
#pragma unroll
      for (int j = 0; j < 4; ++j) R[j] = __builtin_amdgcn_raw_buffer_load_b128(rv, voff + 1024 * j, t * 4096, 2);
      MEMFENCE();
    };
    {
      const unsigned now = clk32();
      PIN4(K0);
      gate(now);
      ldv(K1, w);
      keep(qk(K0), 15);
    }
    const float ML = xrow_max(M) * LOG2E;
    u32x4 pmask;
#pragma unroll
    for (int i = 0; i < 4; ++i) pmask[i] = ((lane & 7) >> 1) == i ? ((lane & 1) ? 0xffff0000u : 0x0000ffffu) : 0u;
    float lsum = 0.f;
    f32x4 O = {0.f, 0.f, 0.f, 0.f};
#pragma unroll 1
    for (int it = 0; it < 16; ++it) {
      const u32x2 t = *(LAS u32x2*)(L + scoff + it * 64);
      const unsigned now = clk32();
      const float p0 = __builtin_amdgcn_exp2f(fmaf(bflo(t[0]), LOG2E, -ML)), p1 = __builtin_amdgcn_exp2f(fmaf(bfhi(t[0]), LOG2E, -ML));
      const float p2 = __builtin_amdgcn_exp2f(fmaf(bflo(t[1]), LOG2E, -ML)), p3 = __builtin_amdgcn_exp2f(fmaf(bfhi(t[1]), LOG2E, -ML));
      lsum += (p0 + p1) + (p2 + p3);
      u32x4 P[4];
      const unsigned pk[4] = {pkbf(p0, p0), pkbf(p1, p1), pkbf(p2, p2), pkbf(p3, p3)};
#pragma unroll
      for (int j = 0; j < 4; ++j) P[j] = u32x4{pk[j] & pmask[0], pk[j] & pmask[1], pk[j] & pmask[2], pk[j] & pmask[3]};
      PIN4(P);
#pragma unroll
      for (int j = 0; j < 4; ++j) O = mfma16(P[j], K1[j], O);
      PIN(O);
      if (it < 15) gate(now);
      ldv(K1, w + 8 * it + 8);
      if (it == 14 && lane == 0) __hip_atomic_fetch_add((LAS unsigned*)(L + CNT_L + 12), 1u, __ATOMIC_RELAXED, __HIP_MEMORY_SCOPE_WORKGROUP);
    }
    lsum = xrow_sum(lsum);
    *(LAS f32x4*)(L + MG_L + w * MG_STR + ((a >> 1) * 128 + 8 * n + 4 * (a & 1)) * 4) = O;
    if ((lane & 55) == 0) *(LAS f32x2*)(L + MG_L + w * MG_STR + 1024 + (lane >> 3) * 8) = f32x2{ML, lsum};
    WG_BARRIER();
  }

  const Tail t = tail_roles(bid);
  if (t.is_wo) {
    u32x4 A0[8], A1[8];
    const int aoff = DN_L + (8 * a + (n >> 2)) * 192 + (n & 3) * 8 + (w < 3 ? w : 0) * 64;
#pragma unroll
    for (int s = 0; s < 8; ++s) { A0[s] = tr_frag(L, aoff + s * 32 * 192, aoff + s * 32 * 192 + 4 * 192); A1[s] = tr_frag(L, aoff + 32 + s * 32 * 192, aoff + 32 + s * 32 * 192 + 4 * 192); }
    const u32x2 xr = *(const u32x2*)(x + (t.is_red ? t.rb * D + t.rc : 0) + n * 4);
    {
      const int r0 = AR_OFF + (t.wkv * 16 + 2 * w) * AR_STR, r1 = r0 + AR_STR;
      u32x4 o0 = ld128(rs, r0 + lane * 16), o1 = ld128(rs, r1 + lane * 16);
      u32x2 m0 = ld64(rs, r0 + 1024 + (lane >> 5) * 8), m1 = ld64(rs, r1 + 1024 + (lane >> 5) * 8);
#pragma unroll 1
      for (unsigned spins = 0; spins <= SPIN_CAP; ++spins) {
        const bool x0 = __any(mxu(mx4(o0), mx2(m0)) == SENT), x1 = __any(mxu(mx4(o1), mx2(m1)) == SENT);
        if (!x0 && !x1) break;
        __builtin_amdgcn_s_sleep(3);
        MEMFENCE();
        if (x0) { o0 = ld128(rs, r0 + lane * 16); m0 = ld64(rs, r0 + 1024 + (lane >> 5) * 8); }
        if (x1) { o1 = ld128(rs, r1 + lane * 16); m1 = ld64(rs, r1 + 1024 + (lane >> 5) * 8); }
      }
      const float ml0 = __uint_as_float(m0[0]), ml1 = __uint_as_float(m1[0]), mm = fmaxf(ml0, ml1);
      const float w0 = __builtin_amdgcn_exp2f(ml0 - mm), w1 = __builtin_amdgcn_exp2f(ml1 - mm);
      const float inv = __builtin_amdgcn_rcpf(w0 * __uint_as_float(m0[1]) + w1 * __uint_as_float(m1[1]));
      const f32x4 o = ((w0 * inv) * __builtin_bit_cast(f32x4, o0) + (w1 * inv) * __builtin_bit_cast(f32x4, o1));
      *(LAS u32x2*)(L + OB_L + w * OB_STR + lane * 8) = u32x2{pkbf(o[0], o[1]), pkbf(o[2], o[3])};
    }
    WG_BARRIER();
    if (w < 3) {
      u32x4 Bf[8];
#pragma unroll
      for (int s = 0; s < 8; ++s) Bf[s] = lds128(L, OB_L + (n & 7) * OB_STR + s * 64 + a * 16);
      f32x4 c0 = {0.f, 0.f, 0.f, 0.f}, c1 = {0.f, 0.f, 0.f, 0.f};
      PIN(c0);
#pragma unroll
      for (int s = 0; s < 8; ++s) { c0 = mfma16(A0[s], Bf[s], c0); c1 = mfma16(A1[s], Bf[s], c1); }
      st128(rs, HP_OFF + ((t.wkv * 8 + (n & 7)) * D + t.ns * 96 + w * 32 + (n >> 3) * 16 + 4 * a) * 4, canon4(n < 8 ? c0 : c1));
      MEMFENCE();
    } else if (w == 4) {
      if (t.ns == 0 && lane < 8) st32(rs, GD_OFF + lane * 128 + t.wkv * 4, 1u);
    } else if (w == 3 && t.is_red) {
      auto reduce16 = [&](int base, bool slow) {
        const int poff = base + ((a * 8 + t.rb) * D + t.rc + n * 4) * 4;
        u32x4 p[4];
#pragma unroll
        for (int i = 0; i < 4; ++i) p[i] = ld128(rs, poff + i * (32 * D * 4));
#pragma unroll 1
        for (unsigned spins = 0; spins <= SPIN_CAP; ++spins) {
          bool miss[4];
#pragma unroll
          for (int i = 0; i < 4; ++i) miss[i] = mx4(p[i]) == SENT;
          const int nm = __popcll(__ballot(miss[0])) + __popcll(__ballot(miss[1])) + __popcll(__ballot(miss[2])) + __popcll(__ballot(miss[3]));
          if (nm == 0) break;
          if (slow && nm > 48) __builtin_amdgcn_s_sleep(12);
          MEMFENCE();
#pragma unroll
          for (int i = 0; i < 4; ++i) if (miss[i]) p[i] = ld128(rs, poff + i * (32 * D * 4));
        }
        f32x4 s = (__builtin_bit_cast(f32x4, p[0]) + __builtin_bit_cast(f32x4, p[1])) + (__builtin_bit_cast(f32x4, p[2]) + __builtin_bit_cast(f32x4, p[3]));
#pragma unroll
        for (int i = 0; i < 4; ++i) s[i] = xrow_sum(s[i]);
        return u32x2{pkbf(s[0], s[1]), pkbf(s[2], s[3])};
      };
      const u32x2 at = reduce16(HP_OFF, true);
      const u32x2 hv = {canonu(pkbf(bflo(xr[0]) + bflo(at[0]), bfhi(xr[0]) + bfhi(at[0]))), canonu(pkbf(bflo(xr[1]) + bflo(at[1]), bfhi(xr[1]) + bfhi(at[1])))};
      {
        const unsigned o0 = (unsigned)__builtin_amdgcn_mov_dpp((int)hv[0], XOR1, 0xf, 0xf, true), o1 = (unsigned)__builtin_amdgcn_mov_dpp((int)hv[1], XOR1, 0xf, 0xf, true);
        const bool odd = n & 1;
        st128(rs, H_OFF + (2 * a + (n & 1)) * H_REP + (t.rb * D + t.rc) * 2 + (n >> 1) * 16,
              u32x4{odd ? o0 : hv[0], odd ? o1 : hv[1], odd ? hv[0] : o0, odd ? hv[1] : o1});
      }
      MEMFENCE();
      const u32x2 y = reduce16(FP_OFF, false);
      if (a == 0) *(u32x2*)(out + t.rb * D + t.rc + n * 4) =
          u32x2{pkbf(bflo(hv[0]) + bflo(y[0]), bfhi(hv[0]) + bfhi(y[0])), pkbf(bflo(hv[1]) + bflo(y[1]), bfhi(hv[1]) + bfhi(y[1]))};
    }
    return;
  }
  if (!t.is_ffn) return;

  WG_BARRIER();
#pragma unroll 1
  for (int i = w; i < 72; i += 8) {
    const int row = i * 8 + (lane >> 3), c = (lane & 7) ^ (((row >> 1) & 1) | ((row >> 2) & 2));
    dma16((const char*)(c < 4 ? wg : wu) + (row * (FF * 2) + t.f * 64 + (c & 3) * 16), lbase + GU_L + i * 1024);
  }
#pragma unroll 1
  for (int i = w; i < 36; i += 8)
    dma16((const char*)wd + ((t.kg * 288 + i * 8 + (lane >> 3)) * 1152 + t.nsl * 128 + (lane & 7) * 16), lbase + DN_L + i * 1024);
  if (w < 4) VMWAIT(5); else VMWAIT(4);
  WG_BARRIER();
  auto get_h = [&]() {
    const int hoff = H_OFF + xcd * H_REP + w * D * 2;
    if (w == 7) {
#pragma unroll 1
      for (unsigned spins = 0; spins <= SPIN_CAP; ++spins) {
        MEMFENCE();
        const unsigned gv = ld32(rs, GD_OFF + xcd * 128 + n * 4);
        if (!__any(gv == SENT)) break;
        __builtin_amdgcn_s_sleep(4);
      }
    }
    WG_BARRIER();
    u32x4 ha = ld128(rs, hoff + lane * 16);
    unsigned hb = ld32(rs, hoff + 1024 + (lane & 31) * 4);
#pragma unroll 1
    for (unsigned spins = 0; spins <= SPIN_CAP; ++spins) {
      const bool ma = mx4(ha) == SENT, mb = hb == SENT;
      if (!__any(ma || mb)) break;
      MEMFENCE();
      if (ma) ha = ld128(rs, hoff + lane * 16);
      if (mb) hb = ld32(rs, hoff + 1024 + (lane & 31) * 4);
    }
    VMWAIT(0);
    ln_row(ha, hb, lane, L + XN_L + w * XN_STR);
  };
  if (w < 4) {
    u32x4 AG[18];
    const int p = n & 3, q = n >> 2;
    const int aoff = GU_L + (8 * a + q) * 128 + (((p >> 1) * 4 + w) ^ ((q >> 1) | ((a & 1) << 1))) * 16 + (p & 1) * 8;
#pragma unroll
    for (int s = 0; s < 18; ++s) AG[s] = tr_frag(L, aoff + s * 4096, aoff + s * 4096 + 512);
    get_h();
    WG_BARRIER();
    u32x4 Bq[18];
#pragma unroll
    for (int s = 0; s < 18; ++s) Bq[s] = lds128(L, XN_L + (n & 7) * XN_STR + a * 16 + s * 64);
    f32x4 acc = {0.f, 0.f, 0.f, 0.f};
    PIN(acc);
#pragma unroll
    for (int s = 0; s < 18; ++s) acc = mfma16(AG[s], Bq[s], acc);
    const bool hi = lane >= 32, n8 = n >= 8;
    const float lo2 = n8 ? acc[1] : acc[0], hi2 = n8 ? acc[3] : acc[2];
    const float keep = hi ? hi2 : lo2, give = hi ? lo2 : hi2;
    auto sw = __builtin_amdgcn_permlane32_swap(__float_as_uint(give), __float_as_uint(give), false, false);
    const float got = __uint_as_float(hi ? sw[0] : sw[1]);
    const float g = rbf(hi ? got : keep), uu = rbf(hi ? keep : got);
    const float z = 0.7978845608028654f * (g + 0.044715f * g * g * g);
    const float ge = g * __builtin_amdgcn_rcpf(1.0f + __builtin_amdgcn_exp2f(-2.0f * 1.4426950408889634f * z));
    const float act = rbf(ge) * uu;
    const float nb = dpp<ROR8>(act);
    const unsigned d0 = canonu(pkbf(act, nb));
    auto s32 = __builtin_amdgcn_permlane32_swap(d0, d0, false, false);
    auto sa = __builtin_amdgcn_permlane16_swap(s32[0], s32[0], false, false);
    auto sb = __builtin_amdgcn_permlane16_swap(s32[1], s32[1], false, false);
    if (lane < 8) st128(rs, AC_OFF + t.f * 512 + w * 128 + lane * 16, u32x4{sa[0], sb[0], sa[1], sb[1]});
    MEMFENCE();
  } else if (w < 6) {
    get_h();
    WG_BARRIER();
    u32x4 AD[18];
    const int aoff = DN_L + (8 * a + (n >> 2)) * 128 + (n & 3) * 8 + (w - 4) * 64;
#pragma unroll
    for (int s = 0; s < 9; ++s) { AD[2 * s] = tr_frag(L, aoff + s * 4096, aoff + s * 4096 + 512); AD[2 * s + 1] = tr_frag(L, aoff + 32 + s * 4096, aoff + 32 + s * 4096 + 512); }
    const int boff = AC_OFF + t.kg * 9 * 512 + a * 128 + (n & 7) * 16;
    u32x4 Bf[9];
    __builtin_amdgcn_s_sleep(12);
#pragma unroll
    for (int s = 0; s < 9; ++s) Bf[s] = ld128(rs, boff + s * 512);
#pragma unroll 1
    for (unsigned spins = 0; spins <= SPIN_CAP; ++spins) {
      bool miss[9]; bool any = false;
#pragma unroll
      for (int s = 0; s < 9; ++s) { miss[s] = __any(mx4(Bf[s]) == SENT); any |= miss[s]; }
      if (!any) break;
      MEMFENCE();
#pragma unroll
      for (int s = 0; s < 9; ++s) if (miss[s]) Bf[s] = ld128(rs, boff + s * 512);
    }
    f32x4 c0 = {0.f, 0.f, 0.f, 0.f}, c1 = {0.f, 0.f, 0.f, 0.f};
#pragma unroll
    for (int s = 0; s < 9; ++s) { c0 = mfma16(AD[2 * s], Bf[s], c0); c1 = mfma16(AD[2 * s + 1], Bf[s], c1); }
    st128(rs, FP_OFF + ((t.kg * 8 + (n & 7)) * D + t.nsl * 64 + (w - 4) * 32 + (n >> 3) * 16 + 4 * a) * 4, canon4(n < 8 ? c0 : c1));
    MEMFENCE();
  } else {
    get_h();
    WG_BARRIER();
  }
}

extern "C" void kernel_launch(void* const* d_in, const int* in_sizes, int n_in,
                              void* d_out, int out_size, void* d_ws, size_t ws_size,
                              hipStream_t stream) {
  fused_block<<<NWG, NTHR, 0, stream>>>((const u16*)d_in[0], (const u16*)d_in[1], (const u16*)d_in[2], (const u16*)d_in[3],
                                        (const u16*)d_in[4], (const u16*)d_in[6], (const u16*)d_in[7], (const u16*)d_in[8],
                                        (u16*)d_out, (unsigned char*)d_ws);
}
```

```cpp
#include <hip/hip_runtime.h>
#include <stdint.h>

#define LAS __attribute__((address_space(3)))
typedef unsigned short u16;
typedef unsigned long long u64;
typedef unsigned u32x4 __attribute__((ext_vector_type(4)));
typedef unsigned u32x2 __attribute__((ext_vector_type(2)));
typedef float f32x4 __attribute__((ext_vector_type(4)));
typedef float f32x2 __attribute__((ext_vector_type(2)));
typedef short s16x8 __attribute__((ext_vector_type(8)));
typedef short s16x4 __attribute__((ext_vector_type(4)));
typedef __bf16 bf16x2 __attribute__((ext_vector_type(2)));

#define HPACE 3
constexpr int NPL = 2;
constexpr int PACE = 111;
constexpr int POFF = 100;
constexpr int QDELAY = 520;
constexpr int CFT = 2600;
constexpr int CFR = 2;

constexpr int D = 576, SK = 4096, FF = 4608, NQ = 4096;
constexpr int NWG = 256, NTHR = 576, NFFN = 144;

constexpr int EPOCH_OFF = 0;
constexpr int CF_OFF = 4096;
constexpr int QG_OFF = CF_OFF + 16 * 2048;
constexpr int AR_OFF = QG_OFF + 8 * 16 * 8192;
constexpr int AR_STR = 1152;
constexpr int HP_OFF = AR_OFF + NWG * AR_STR;
constexpr int H_OFF = HP_OFF + 16 * 8 * D * 4;
constexpr int H_REP = 8 * D * 2;
constexpr int AC_OFF = H_OFF + 8 * H_REP;
constexpr int FP_OFF = AC_OFF + NFFN * 512;
constexpr int GD_OFF = FP_OFF + 16 * 8 * D * 4;
constexpr int WS_BYTES = GD_OFF + 8 * 128;
constexpr unsigned SENT = 0xFFFFFFFFu;

constexpr int WQ_L = 0;
constexpr int DN_L = 18432;
constexpr int GU_L = DN_L + 36864;
constexpr int KP_L = GU_L + 12288;
constexpr int XN_L = GU_L + 77824;
constexpr int XN_STR = 1168;
constexpr int Q_L = XN_L + 8 * XN_STR;
constexpr int MG_L = Q_L + 512;
constexpr int MG_STR = 1040;
constexpr int CNT_L = MG_L + 8 * MG_STR;
constexpr int LDS_BYTES = CNT_L + 16;
constexpr int OB_L = XN_L;
constexpr int OB_STR = 528;
constexpr unsigned QREADY = 1u << 30;
static_assert(LDS_BYTES <= 160 * 1024, "LDS");

constexpr unsigned SPIN_CAP = 1u << 18;

__device__ __forceinline__ float bflo(unsigned u) { return __uint_as_float(u << 16); }
__device__ __forceinline__ float bfhi(unsigned u) { return __uint_as_float(u & 0xffff0000u); }
__device__ __forceinline__ unsigned pkbf(float lo, float hi) { return __builtin_bit_cast(unsigned, __builtin_convertvector(f32x2{lo, hi}, bf16x2)); }
__device__ __forceinline__ float rbf(float x) { return bflo(pkbf(x, x)); }
__device__ __forceinline__ f32x4 mfma16(u32x4 a, u32x4 b, f32x4 c) {
  return __builtin_amdgcn_mfma_f32_16x16x32_bf16(__builtin_bit_cast(s16x8, a), __builtin_bit_cast(s16x8, b), c, 0, 0, 0);
}
template <int CTRL> __device__ __forceinline__ float dpp(float x) {
  return __builtin_bit_cast(float, __builtin_amdgcn_mov_dpp(__builtin_bit_cast(int, x), CTRL, 0xf, 0xf, true));
}
constexpr int XOR1 = 0xB1, XOR2 = 0x4E, XOR7 = 0x141, ROR8 = 0x128;
__device__ __forceinline__ float xrow_max(float x) {
  auto s = __builtin_amdgcn_permlane16_swap(__float_as_uint(x), __float_as_uint(x), false, false);
  x = fmaxf(__uint_as_float(s[0]), __uint_as_float(s[1]));
  auto t = __builtin_amdgcn_permlane32_swap(__float_as_uint(x), __float_as_uint(x), false, false);
  return fmaxf(__uint_as_float(t[0]), __uint_as_float(t[1]));
}
__device__ __forceinline__ float xrow_sum(float x) {
  auto s = __builtin_amdgcn_permlane16_swap(__float_as_uint(x), __float_as_uint(x), false, false);
  x = __uint_as_float(s[0]) + __uint_as_float(s[1]);
  auto t = __builtin_amdgcn_permlane32_swap(__float_as_uint(x), __float_as_uint(x), false, false);
  return __uint_as_float(t[0]) + __uint_as_float(t[1]);
}
__device__ __forceinline__ float row_sum(float x) {
  x += dpp<XOR1>(x); x += dpp<XOR2>(x); x += dpp<XOR7>(x); x += dpp<ROR8>(x); return x;
}
__device__ __forceinline__ float wave_sum(float x) { return xrow_sum(row_sum(x)); }
__device__ __forceinline__ unsigned clk32() { return (unsigned)__builtin_amdgcn_s_memrealtime(); }

#define WG_BARRIER() do { asm volatile("s_waitcnt lgkmcnt(0)" ::: "memory"); __builtin_amdgcn_s_barrier(); asm volatile("" ::: "memory"); } while (0)
#define VMWAIT_(n) asm volatile("s_waitcnt vmcnt(" #n ")" ::: "memory")
#define VMWAIT(n) VMWAIT_(n)
#define MEMFENCE() asm volatile("" ::: "memory")
#define PIN(v) asm volatile("" : "+v"(v) :: "memory")
#define PIN4(R) asm volatile("" : "+v"(R[0]), "+v"(R[1]), "+v"(R[2]), "+v"(R[3]) :: "memory")

#define DMA_ASM(INSN) \
  unsigned keep; \
  asm volatile("s_mov_b32 %0, m0\n\ts_mov_b32 m0, %2\n\ts_nop 0\n\t" INSN "\n\ts_mov_b32 m0, %0" : "=&s"(keep) : "v"(gsrc), "s"(lds_dst) : "memory")
__device__ __forceinline__ void dma16(const void* gsrc, unsigned lds_dst) { DMA_ASM("global_load_lds_dwordx4 %1, off"); }
__device__ __forceinline__ void dma16nt(const void* gsrc, unsigned lds_dst) { DMA_ASM("global_load_lds_dwordx4 %1, off nt"); }
__device__ __forceinline__ void dma4(const void* gsrc, unsigned lds_dst) { DMA_ASM("global_load_lds_dword %1, off"); }

typedef __amdgpu_buffer_rsrc_t rsrc_t;
__device__ __forceinline__ u32x4 ld128(rsrc_t rs, int off) { return __builtin_amdgcn_raw_buffer_load_b128(rs, off, 0, 16); }
__device__ __forceinline__ u32x2 ld64(rsrc_t rs, int off) { return __builtin_amdgcn_raw_buffer_load_b64(rs, off, 0, 16); }
__device__ __forceinline__ unsigned ld32(rsrc_t rs, int off) { return __builtin_amdgcn_raw_buffer_load_b32(rs, off, 0, 16); }
__device__ __forceinline__ void st128(rsrc_t rs, int off, u32x4 v) { __builtin_amdgcn_raw_buffer_store_b128(v, rs, off, 0, 16); }
__device__ __forceinline__ void st64(rsrc_t rs, int off, u32x2 v) { __builtin_amdgcn_raw_buffer_store_b64(v, rs, off, 0, 16); }
__device__ __forceinline__ void st32(rsrc_t rs, int off, unsigned v) { __builtin_amdgcn_raw_buffer_store_b32(v, rs, off, 0, 16); }
__device__ __forceinline__ unsigned canonu(unsigned u) { return u < 0xFFFFFFFEu ? u : 0xFFFFFFFEu; }
__device__ __forceinline__ unsigned canon(float v) { return canonu(__float_as_uint(v)); }
__device__ __forceinline__ u32x4 canon4(f32x4 v) { return u32x4{canon(v[0]), canon(v[1]), canon(v[2]), canon(v[3])}; }
__device__ __forceinline__ unsigned mxu(unsigned a, unsigned b) { return a > b ? a : b; }
__device__ __forceinline__ unsigned mx4(u32x4 v) { return mxu(mxu(v[0], v[1]), mxu(v[2], v[3])); }
__device__ __forceinline__ unsigned mx2(u32x2 v) { return mxu(v[0], v[1]); }
__device__ __forceinline__ unsigned lds_add(LAS unsigned char* L, int off, unsigned v, int lane) {
  unsigned r = 0;
  if (lane == 0) r = __hip_atomic_fetch_add((LAS unsigned*)(L + off), v, __ATOMIC_RELAXED, __HIP_MEMORY_SCOPE_WORKGROUP);
  return __builtin_amdgcn_readfirstlane(r);
}

__device__ __forceinline__ u32x4 lds128(LAS unsigned char* L, int off) { return *(LAS u32x4*)(L + off); }
__device__ __forceinline__ u32x4 tr_frag(LAS unsigned char* L, int off, int off2) {
  const s16x4 a = __builtin_amdgcn_ds_read_tr16_b64_v4i16((LAS s16x4*)(L + off));
  const s16x4 b = __builtin_amdgcn_ds_read_tr16_b64_v4i16((LAS s16x4*)(L + off2));
  const u32x2 x = __builtin_bit_cast(u32x2, a), y = __builtin_bit_cast(u32x2, b);
  return u32x4{x[0], x[1], y[0], y[1]};
}

__device__ __forceinline__ void ln_row(u32x4 xa, unsigned xb, int lane, LAS unsigned char* dst) {
  float v[10];
#pragma unroll
  for (int i = 0; i < 4; ++i) { v[2 * i] = bflo(xa[i]); v[2 * i + 1] = bfhi(xa[i]); }
  v[8] = bflo(xb); v[9] = bfhi(xb);
  const bool tail = lane < 32;
  const float x0 = __uint_as_float(__builtin_amdgcn_readfirstlane(xa[0] << 16));
  float d[10];
#pragma unroll
  for (int i = 0; i < 10; ++i) d[i] = v[i] - x0;
  if (!tail) { d[8] = 0.f; d[9] = 0.f; }
  float s1 = (((d[0] + d[1]) + (d[2] + d[3])) + ((d[4] + d[5]) + (d[6] + d[7]))) + (d[8] + d[9]);
  float qa = d[0] * d[0], qb = d[1] * d[1];
#pragma unroll
  for (int i = 2; i < 10; i += 2) { qa = fmaf(d[i], d[i], qa); qb = fmaf(d[i + 1], d[i + 1], qb); }
  float s2 = qa + qb;
  s1 = wave_sum(s1); s2 = wave_sum(s2);
  const float md = s1 * (1.f / D);
  const float var = fmaxf(fmaf(-md, md, s2 * (1.f / D)), 0.f);
  const float mb = rbf(x0 + md);
  const float sd = rbf(__builtin_amdgcn_sqrtf(rbf(rbf(var) + 1e-5f)));
  const float r = __builtin_amdgcn_rcpf(sd);
  unsigned y[5];
#pragma unroll
  for (int i = 0; i < 5; ++i) { const unsigned c = pkbf(v[2 * i] - mb, v[2 * i + 1] - mb); y[i] = pkbf(bflo(c) * r, bfhi(c) * r); }
  *(LAS u32x4*)(dst + lane * 16) = u32x4{y[0], y[1], y[2], y[3]};
  if (tail) *(LAS unsigned*)(dst + 1024 + lane * 4) = y[4];
}

struct Tail {
  bool is_ffn, is_wo, is_red;
  int f, wkv, ns, kg, nsl, rb, rc;
};
__device__ __forceinline__ Tail tail_roles(int bid) {
  asm volatile("" : "+s"(bid));
  const int xcd = bid & 7, jj = bid >> 3;
  Tail t;
  t.is_ffn = jj < 18; t.is_wo = jj >= 18 && jj < 30;
  t.f = xcd * 18 + jj;
  t.kg = 2 * xcd + (jj >= 9 ? 1 : 0); t.nsl = jj >= 9 ? jj - 9 : jj;
  t.wkv = 2 * xcd + (jj >= 24 ? 1 : 0); t.ns = jj >= 24 ? jj - 24 : jj - 18;
  const int u = xcd * 12 + (jj - 18), rr = (u >> 2) * 3 + (u & 3);
  t.is_red = t.is_wo && (u & 3) != 3;
  t.rb = rr / 9; t.rc = (rr - t.rb * 9) * 64;
  return t;
}

__global__ void __launch_bounds__(NTHR, 3)
fused_block(const u16* __restrict__ x, const u16* __restrict__ kc, const u16* __restrict__ vc, const u16* __restrict__ wq,
            const u16* __restrict__ wo, const u16* __restrict__ wg, const u16* __restrict__ wu, const u16* __restrict__ wd,
            u16* __restrict__ out, unsigned char* __restrict__ ws) {
  __shared__ __attribute__((aligned(16))) unsigned char lds_raw[LDS_BYTES];
  LAS unsigned char* L = (LAS unsigned char*)lds_raw;
  const unsigned lbase = (unsigned)(size_t)L;
  const int tid = threadIdx.x, lane = tid & 63, w = __builtin_amdgcn_readfirstlane(tid >> 6);
  const int bid = blockIdx.x, xcd = bid & 7, jj = bid >> 3;
  const int kvh = 2 * xcd + (jj >> 4), mem = jj & 15, ab = mem >> 1, split = mem & 1;
  const int a = lane >> 4, n = lane & 15;
  asm volatile("" :: "s"(x), "s"(kc), "s"(vc), "s"(wq), "s"(wo), "s"(wg), "s"(wu), "s"(wd), "s"(out), "s"(ws));
  const rsrc_t rs = __builtin_amdgcn_make_buffer_rsrc((void*)ws, 0, WS_BYTES, 0x00020000);
  auto wo_job = [&](const Tail& t, int i) {
    const unsigned ci = i * 64 + lane, row = (ci * 87382u) >> 20, e = ci - row * 12u;
    dma16((const char*)(wo + (size_t)t.wkv * 256 * D + t.ns * 96) + (row * 1152u + e * 16u), lbase + DN_L + i * 1024);
  };

  if (w == 8) {
    __builtin_amdgcn_s_setprio(3);
    __builtin_amdgcn_s_waitcnt(0x0F70);
    const int cb = mem & 3, kq = mem >> 2;
#pragma unroll 1
    for (int i = 0; i < 18; ++i) {
      const int row = i * 8 + (lane >> 3), c = (lane & 7) ^ ((row & 2) | ((row >> 1) & 4));
      dma16nt((const char*)wq + ((kq * 144 + row) * (NQ * 2) + kvh * 512 + cb * 128 + c * 16), lbase + WQ_L + i * 1024);
    }
    const u32x2 ep = ld64(rs, EPOCH_OFF);
    const unsigned tstart = clk32();
    if (lane == 0) *(LAS u32x4*)(L + CNT_L) = u32x4{0u, 0u, 0u, 0u};
    WG_BARRIER();
    const u64 tag = (((u64)ep[1] << 32) | ep[0]) + 1ull;
    const unsigned tag_lo = (unsigned)tag, tag_hi = (unsigned)(tag >> 32);
    const unsigned tg0 = tag_lo, tg1 = tag_lo * 0x9E3779B1u + tag_hi;
    VMWAIT(0);
    u32x4 A[4][5];
    {
      const int p = n & 3, q = n >> 2, sw = (q & 2) | ((a & 1) << 2);
#pragma unroll
      for (int T = 0; T < 4; ++T) {
        const int cofs = ((2 * T + (p >> 1)) ^ sw) * 16 + (p & 1) * 8;
#pragma unroll
        for (int s = 0; s < 5; ++s) {
          const int o = WQ_L + (32 * s + 8 * (s < 4 ? a : (a & 1)) + q) * 128 + cofs;
          A[T][s] = tr_frag(L, o, o + 512);
        }
      }
    }
#pragma unroll 1
    for (unsigned spins = 0; spins < (SPIN_CAP << 4); ++spins) {
      if (__builtin_amdgcn_readfirstlane(*(volatile LAS unsigned*)(L + CNT_L + 8)) == 8u) break;
    }
    MEMFENCE();
    {
      u32x4 Bq[5];
#pragma unroll
      for (int s = 0; s < 5; ++s) Bq[s] = lds128(L, XN_L + (n & 7) * XN_STR + kq * 288 + s * 64 + (s < 4 ? a : (a & 1)) * 16);
      if (a >= 2) Bq[4] = u32x4{0u, 0u, 0u, 0u};
      f32x4 c[4];
#pragma unroll
      for (int T = 0; T < 4; ++T) c[T] = f32x4{0.f, 0.f, 0.f, 0.f};
#pragma unroll
      for (int s = 0; s < 5; ++s)
#pragma unroll
        for (int T = 0; T < 4; ++T) c[T] = mfma16(A[T][s], Bq[s], c[T]);
      const int so = QG_OFF + ((n & 7) * 16 + kvh) * 8192 + kq * 2048 + (cb * 64 + 4 * a + 2 * (n >> 3)) * 8;
#pragma unroll
      for (int T = 0; T < 4; ++T)
        st128(rs, so + T * 128, u32x4{__float_as_uint(n < 8 ? c[T][0] : c[T][2]), tg0, __float_as_uint(n < 8 ? c[T][1] : c[T][3]), tg1});
    }
    MEMFENCE();
    const Tail t = tail_roles(bid);
    const int arec = kvh * 16 + mem;
    {
      const u32x4 sent = {SENT, SENT, SENT, SENT};
      st128(rs, AR_OFF + arec * AR_STR + lane * 16, sent);
      if (lane == 0) st128(rs, AR_OFF + arec * AR_STR + 1024, sent);
      if (t.is_wo) {
#pragma unroll 1
        for (int c = lane; c < 192; c += 64) st128(rs, HP_OFF + ((t.wkv * 8 + c / 24) * D + t.ns * 96) * 4 + (c % 24) * 16, sent);
        if (t.ns == 0 && lane < 8) st32(rs, GD_OFF + lane * 128 + t.wkv * 4, SENT);
        if (t.is_red) st128(rs, H_OFF + (lane >> 3) * H_REP + (t.rb * D + t.rc) * 2 + (lane & 7) * 16, sent);
      } else if (t.is_ffn) {
        if (lane < 32) st128(rs, AC_OFF + t.f * 512 + lane * 16, sent);
        st128(rs, FP_OFF + ((t.kg * 8 + (lane >> 4)) * D + t.nsl * 64) * 4 + (lane & 15) * 16, sent);
        st128(rs, FP_OFF + ((t.kg * 8 + 4 + (lane >> 4)) * D + t.nsl * 64) * 4 + (lane & 15) * 16, sent);
      }
      MEMFENCE();
    }
    {
      const int qoff = QG_OFF + (ab * 16 + kvh) * 8192 + lane * 32;
      u32x4 g[8];
      while ((int)(clk32() - tstart) < QDELAY) __builtin_amdgcn_s_sleep(2);
#pragma unroll
      for (int i = 0; i < 8; ++i) g[i] = ld128(rs, qoff + (i >> 1) * 2048 + (i & 1) * 16);
#pragma unroll 1
      for (unsigned spins = 0; spins <= SPIN_CAP; ++spins) {
        bool bad[4];
#pragma unroll
        for (int k = 0; k < 4; ++k) bad[k] = ((g[2 * k][1] ^ tg0) | (g[2 * k][3] ^ tg1) | (g[2 * k + 1][1] ^ tg0) | (g[2 * k + 1][3] ^ tg1)) != 0u;
        if (!__any(bad[0] || bad[1] || bad[2] || bad[3])) break;
        __builtin_amdgcn_s_sleep(16);
        MEMFENCE();
#pragma unroll
        for (int k = 0; k < 4; ++k) if (bad[k]) { g[2 * k] = ld128(rs, qoff + k * 2048); g[2 * k + 1] = ld128(rs, qoff + k * 2048 + 16); }
      }
      float qv[4];
#pragma unroll
      for (int e = 0; e < 4; ++e) {
        const int h = e >> 1, d = (e & 1) * 2;
        qv[e] = (__uint_as_float(g[h][d]) + __uint_as_float(g[2 + h][d])) + (__uint_as_float(g[4 + h][d]) + __uint_as_float(g[6 + h][d]));
      }
      *(LAS u32x2*)(L + Q_L + lane * 8) = u32x2{pkbf(qv[0], qv[1]), pkbf(qv[2], qv[3])};
    }
    asm volatile("s_waitcnt lgkmcnt(0)" ::: "memory");
    lds_add(L, CNT_L + 4, QREADY, lane);
    VMWAIT(0);
    if (lane < CFR) st64(rs, CF_OFF + lane * 2048 + bid * 8, u32x2{tag_lo, tag_hi});
    MEMFENCE();
    if (t.is_wo) {
      for (;;) {
        const int i = (int)(lds_add(L, CNT_L + 4, 1u, lane) & (QREADY - 1));
        if (i >= 48) break;
        wo_job(t, i);
        VMWAIT(HPACE);
      }
    }
    while ((int)(clk32() - tstart) < CFT) __builtin_amdgcn_s_sleep(32);
    for (unsigned spins = 0;; ++spins) {
      MEMFENCE();
      const u32x4 d0 = ld128(rs, CF_OFF + (bid & (CFR - 1)) * 2048 + lane * 32), d1 = ld128(rs, CF_OFF + (bid & (CFR - 1)) * 2048 + lane * 32 + 16);
      const unsigned bad = (d0[0] ^ tag_lo) | (d0[1] ^ tag_hi) | (d0[2] ^ tag_lo) | (d0[3] ^ tag_hi) |
                           (d1[0] ^ tag_lo) | (d1[1] ^ tag_hi) | (d1[2] ^ tag_lo) | (d1[3] ^ tag_hi);
      if (__all(bad == 0) || spins > SPIN_CAP) break;
      __builtin_amdgcn_s_sleep(64);
    }
    if (bid == 0 && lane == 0) st64(rs, EPOCH_OFF, u32x2{tag_lo, tag_hi});
    VMWAIT(0);
#pragma unroll 1
    for (unsigned spins = 0; spins < (SPIN_CAP << 6); ++spins) {
      if (__builtin_amdgcn_readfirstlane(*(volatile LAS unsigned*)(L + CNT_L + 12)) >= 8u) break;
      __builtin_amdgcn_s_sleep(2);
    }
#pragma unroll 1
    for (int k = 0; k < 324; k += 64) {
      const int e0 = k + lane, e = e0 < 324 ? e0 : 323;
      const int i = jj * 324 + e, m = i >= 5184 ? 1 : 0, r9 = i - m * 5184, r = r9 / 9, c = r9 - r * 9;
      dma4((const char*)(m ? wu : wg) + (r * (FF * 2) + xcd * 1152 + c * 128), lbase + WQ_L);
    }
    WG_BARRIER();
    f32x2 ml[8]; f32x4 ow[8];
#pragma unroll
    for (int i = 0; i < 8; ++i) { ml[i] = *(LAS f32x2*)(L + MG_L + i * MG_STR + 1024 + (lane >> 5) * 8); ow[i] = *(LAS f32x4*)(L + MG_L + i * MG_STR + lane * 16); }
    __builtin_amdgcn_sched_barrier(0);
    float mxl = ml[0][0];
#pragma unroll
    for (int i = 1; i < 8; ++i) mxl = fmaxf(mxl, ml[i][0]);
    f32x4 o = {0.f, 0.f, 0.f, 0.f}; float ls = 0.f;
#pragma unroll
    for (int i = 0; i < 8; ++i) { const float wgt = __builtin_amdgcn_exp2f(ml[i][0] - mxl); o += wgt * ow[i]; ls = fmaf(wgt, ml[i][1], ls); }
    st128(rs, AR_OFF + arec * AR_STR + lane * 16, canon4(o));
    {
      auto sm = __builtin_amdgcn_permlane32_swap(__float_as_uint(mxl), __float_as_uint(mxl), false, false);
      auto sl = __builtin_amdgcn_permlane32_swap(__float_as_uint(ls), __float_as_uint(ls), false, false);
      if (lane == 0) st128(rs, AR_OFF + arec * AR_STR + 1024, u32x4{canonu(sm[0]), canonu(sl[0]), canonu(sm[1]), canonu(sl[1])});
    }
    MEMFENCE();
    if (t.is_wo || t.is_ffn) WG_BARRIER();
    return;
  }

  {
    const size_t kvoff = ((size_t)(ab * 16 + kvh) * SK + split * 2048) * 128;
    const rsrc_t rk = __builtin_amdgcn_make_buffer_rsrc((void*)(kc + kvoff), 0, 2048 * 256, 0x00020000);
    const int koff = (4 * (n & 3) + ((n >> 2) & 1)) * 256 + (n >> 3) * 64 + a * 16, voff = lane * 16;
    auto ldk = [&](u32x4 (&R)[4], int t) {
#pragma unroll
      for (int u = 0; u < 4; ++u) R[u] = __builtin_amdgcn_raw_buffer_load_b128(rk, koff + (u & 1) * 512 + (u >> 1) * 128, t * 4096, 2);
      MEMFENCE();
    };
    u32x4 K0[4], K1[4], K2[4], K3[4], K4[4], K5[4];
    const u32x4 xa = *(const u32x4*)(x + w * D + lane * 8);
    const unsigned xb = *(const unsigned*)(x + w * D + 512 + (lane & 31) * 2);
    MEMFENCE();
    ldk(K0, w);
    unsigned due = clk32() + (POFF + (6 + NPL) * PACE);
    ldk(K1, w + 8);
    WG_BARRIER();
    ln_row(xa, xb, lane, L + XN_L + w * XN_STR);
    asm volatile("s_waitcnt lgkmcnt(0)" ::: "memory");
    lds_add(L, CNT_L + 8, 1u, lane);
    VMWAIT(2); ldk(K2, w + 16);
    VMWAIT(0); ldk(K3, w + 24);
    VMWAIT(0); ldk(K4, w + 32);
    VMWAIT(0); ldk(K5, w + 40);
#pragma unroll 1
    for (int i = 0; i < NPL; ++i) {
      const char* src = (const char*)(kc + kvoff) + ((w + 48 + 8 * i) * 4096 + koff);
      VMWAIT(0);
#pragma unroll
      for (int u = 0; u < 4; ++u) dma16nt(src + ((u & 1) * 512 + (u >> 1) * 128), lbase + KP_L + (w * 2 + i) * 4096 + u * 1024);
    }
    {
      const Tail t = tail_roles(bid);
#pragma unroll 1
      for (unsigned spins = 0; spins < (SPIN_CAP << 4); ++spins) {
        if (__builtin_amdgcn_readfirstlane(*(volatile LAS unsigned*)(L + CNT_L + 4)) & QREADY) break;
        VMWAIT(2);
        int i = 48;
        if (t.is_wo) i = (int)(lds_add(L, CNT_L + 4, 1u, lane) & (QREADY - 1));
        if (i < 48) wo_job(t, i); else __builtin_amdgcn_s_sleep(4);
      }
    }
    MEMFENCE();
    u32x4 Qf[4];
#pragma unroll
    for (int s = 0; s < 4; ++s) Qf[s] = lds128(L, Q_L + (n >> 3) * 256 + s * 64 + a * 16);
    const float SCALE = 0.088388347648318440f, LOG2E = 1.4426950408889634f;
    const int scoff = MG_L + w * MG_STR + a * 16 + (n >> 3) * 8;
    float M = -INFINITY;
    auto qk = [&](u32x4 (&K)[4]) {
      u32x4 Kf[4];
#pragma unroll
      for (int h = 0; h < 2; ++h)
#pragma unroll
        for (int i = 0; i < 4; ++i) {
          Kf[2 * h][i] = __builtin_amdgcn_update_dpp(K[2 * h][i], K[2 * h + 1][i], ROR8, 0xf, 0xc, false);
          Kf[2 * h + 1][i] = __builtin_amdgcn_update_dpp(K[2 * h + 1][i], K[2 * h][i], ROR8, 0xf, 0x3, false);
        }
      f32x4 S = {0.f, 0.f, 0.f, 0.f};
#pragma unroll
      for (int s = 0; s < 4; ++s) S = mfma16(Kf[s], Qf[s], S);
      PIN(S);
      return S;
    };
    auto keep = [&](f32x4 S, int it) {
      const unsigned r0 = pkbf(S[0], S[1]), r1 = pkbf(S[2], S[3]);
      const unsigned t0 = pkbf(bflo(r0) * SCALE, bfhi(r0) * SCALE), t1 = pkbf(bflo(r1) * SCALE, bfhi(r1) * SCALE);
      M = fmaxf(fmaxf(M, fmaxf(bflo(t0), bfhi(t0))), fmaxf(bflo(t1), bfhi(t1)));
      if ((n & 7) == 0) *(LAS u32x2*)(L + scoff + it * 64) = u32x2{t0, t1};
    };
    auto gate = [&](unsigned now) {
      due += PACE;
      if ((int)(now - due) < 100) __builtin_amdgcn_s_sleep(6);
      if ((int)(now - due) < 0) __builtin_amdgcn_s_sleep(8);
      while ((int)(now - due) < 0) now = clk32();
    };
    constexpr int NB = 6 + NPL;
    { const f32x4 S = qk(K0); VMWAIT(0); ldk(K0, w + 8 * NB); keep(S, 0); }
    keep(qk(K1), 1); keep(qk(K2), 2); keep(qk(K3), 3); keep(qk(K4), 4);
    VMWAIT(0); ldk(K1, w + 8 * NB + 8);
    keep(qk(K5), 5);
#pragma unroll 1
    for (int i = 0; i < NPL; ++i) {
#pragma unroll
      for (int u = 0; u < 4; ++u) K2[u] = lds128(L, KP_L + (w * 2 + i) * 4096 + u * 1024 + lane * 16);
      keep(qk(K2), 6 + i);
    }
    { const f32x4 S = qk(K0); VMWAIT(0); due += 2 * PACE; ldk(K0, w + 8 * NB + 16); keep(S, NB); }
    keep(qk(K1), NB + 1);
#pragma unroll 1
    for (int it = NB + 2; it < 15; ++it) {
      const unsigned now = clk32();
      const f32x4 S = qk(K0);
      gate(now);
      ldk(K0, w + 8 * it + 8);
      keep(S, it);
    }
    const rsrc_t rv = __builtin_amdgcn_make_buffer_rsrc((void*)(vc + kvoff), 0, 2048 * 256, 0x00020000);
    auto ldv = [&](u32x4 (&R)[4], int t) {
#pragma unroll
      for (int j = 0; j < 4; ++j) { R[j] = __builtin_amdgcn_raw_buffer_load_b128(rv, voff + 1024 * j, t * 4096, 2); MEMFENCE(); }
    };
    {
      const unsigned now = clk32();
      PIN4(K0);
      gate(now);
      ldv(K1, w);
      keep(qk(K0), 15);
    }
    const float ML = xrow_max(M) * LOG2E;
    u32x4 pmask;
#pragma unroll
    for (int i = 0; i < 4; ++i) pmask[i] = ((lane & 7) >> 1) == i ? ((lane & 1) ? 0xffff0000u : 0x0000ffffu) : 0u;
    float lsum = 0.f;
    f32x4 O = {0.f, 0.f, 0.f, 0.f};
#pragma unroll 1
    for (int it = 0; it < 16; ++it) {
      const u32x2 t = *(LAS u32x2*)(L + scoff + it * 64);
      const unsigned now = clk32();
      const float p0 = __builtin_amdgcn_exp2f(fmaf(bflo(t[0]), LOG2E, -ML)), p1 = __builtin_amdgcn_exp2f(fmaf(bfhi(t[0]), LOG2E, -ML));
      const float p2 = __builtin_amdgcn_exp2f(fmaf(bflo(t[1]), LOG2E, -ML)), p3 = __builtin_amdgcn_exp2f(fmaf(bfhi(t[1]), LOG2E, -ML));
      lsum += (p0 + p1) + (p2 + p3);
      u32x4 P[4];
      const unsigned pk[4] = {pkbf(p0, p0), pkbf(p1, p1), pkbf(p2, p2), pkbf(p3, p3)};
#pragma unroll
      for (int j = 0; j < 4; ++j) P[j] = u32x4{pk[j] & pmask[0], pk[j] & pmask[1], pk[j] & pmask[2], pk[j] & pmask[3]};
      PIN4(P);
#pragma unroll
      for (int j = 0; j < 4; ++j) O = mfma16(P[j], K1[j], O);
      PIN(O);
      if (it < 15) gate(now);
      ldv(K1, w + 8 * it + 8);
      if (it == 14 && lane == 0) __hip_atomic_fetch_add((LAS unsigned*)(L + CNT_L + 12), 1u, __ATOMIC_RELAXED, __HIP_MEMORY_SCOPE_WORKGROUP);
    }
    lsum = xrow_sum(lsum);
    *(LAS f32x4*)(L + MG_L + w * MG_STR + ((a >> 1) * 128 + 8 * n + 4 * (a & 1)) * 4) = O;
    if ((lane & 55) == 0) *(LAS f32x2*)(L + MG_L + w * MG_STR + 1024 + (lane >> 3) * 8) = f32x2{ML, lsum};
    WG_BARRIER();
  }

  __builtin_amdgcn_s_waitcnt(0x0F70);
  const Tail t = tail_roles(bid);
  if (t.is_wo) {
    u32x4 A0[8], A1[8];
    const int aoff = DN_L + (8 * a + (n >> 2)) * 192 + (n & 3) * 8 + (w < 3 ? w : 0) * 64;
#pragma unroll
    for (int s = 0; s < 8; ++s) { A0[s] = tr_frag(L, aoff + s * 32 * 192, aoff + s * 32 * 192 + 4 * 192); A1[s] = tr_frag(L, aoff + 32 + s * 32 * 192, aoff + 32 + s * 32 * 192 + 4 * 192); }
    const u32x2 xr = *(const u32x2*)(x + (t.is_red ? t.rb * D + t.rc : 0) + n * 4);
    {
      const int r0 = AR_OFF + (t.wkv * 16 + 2 * w) * AR_STR, r1 = r0 + AR_STR;
      u32x4 o0 = ld128(rs, r0 + lane * 16), o1 = ld128(rs, r1 + lane * 16);
      u32x2 m0 = ld64(rs, r0 + 1024 + (lane >> 5) * 8), m1 = ld64(rs, r1 + 1024 + (lane >> 5) * 8);
#pragma unroll 1
      for (unsigned spins = 0; spins <= SPIN_CAP; ++spins) {
        const bool x0 = __any(mxu(mx4(o0), mx2(m0)) == SENT), x1 = __any(mxu(mx4(o1), mx2(m1)) == SENT);
        if (!x0 && !x1) break;
        __builtin_amdgcn_s_sleep(3);
        MEMFENCE();
        if (x0) { o0 = ld128(rs, r0 + lane * 16); m0 = ld64(rs, r0 + 1024 + (lane >> 5) * 8); }
        if (x1) { o1 = ld128(rs, r1 + lane * 16); m1 = ld64(rs, r1 + 1024 + (lane >> 5) * 8); }
      }
      const float ml0 = __uint_as_float(m0[0]), ml1 = __uint_as_float(m1[0]), mm = fmaxf(ml0, ml1);
      const float w0 = __builtin_amdgcn_exp2f(ml0 - mm), w1 = __builtin_amdgcn_exp2f(ml1 - mm);
      const float inv = __builtin_amdgcn_rcpf(w0 * __uint_as_float(m0[1]) + w1 * __uint_as_float(m1[1]));
      const f32x4 o = ((w0 * inv) * __builtin_bit_cast(f32x4, o0) + (w1 * inv) * __builtin_bit_cast(f32x4, o1));
      *(LAS u32x2*)(L + OB_L + w * OB_STR + lane * 8) = u32x2{pkbf(o[0], o[1]), pkbf(o[2], o[3])};
    }
    WG_BARRIER();
    if (w < 3) {
      u32x4 Bf[8];
#pragma unroll
      for (int s = 0; s < 8; ++s) Bf[s] = lds128(L, OB_L + (n & 7) * OB_STR + s * 64 + a * 16);
      f32x4 c0 = {0.f, 0.f, 0.f, 0.f}, c1 = {0.f, 0.f, 0.f, 0.f};
      PIN(c0);
#pragma unroll
      for (int s = 0; s < 8; ++s) { c0 = mfma16(A0[s], Bf[s], c0); c1 = mfma16(A1[s], Bf[s], c1); }
      st128(rs, HP_OFF + ((t.wkv * 8 + (n & 7)) * D + t.ns * 96 + w * 32 + (n >> 3) * 16 + 4 * a) * 4, canon4(n < 8 ? c0 : c1));
      MEMFENCE();
    } else if (w == 4) {
      if (t.ns == 0 && lane < 8) st32(rs, GD_OFF + lane * 128 + t.wkv * 4, 1u);
    } else if (w == 3 && t.is_red) {
      auto reduce16 = [&](int base, bool slow) {
        const int poff = base + ((a * 8 + t.rb) * D + t.rc + n * 4) * 4;
        u32x4 p[4];
#pragma unroll
        for (int i = 0; i < 4; ++i) p[i] = ld128(rs, poff + i * (32 * D * 4));
#pragma unroll 1
        for (unsigned spins = 0; spins <= SPIN_CAP; ++spins) {
          bool miss[4];
#pragma unroll
          for (int i = 0; i < 4; ++i) miss[i] = mx4(p[i]) == SENT;
          if (!__any(mxu(mxu(mx4(p[0]), mx4(p[1])), mxu(mx4(p[2]), mx4(p[3]))) == SENT)) break;
          if (slow) {
            const int nm = __popcll(__ballot(miss[0])) + __popcll(__ballot(miss[1])) + __popcll(__ballot(miss[2])) + __popcll(__ballot(miss[3]));
            if (nm > 48) __builtin_amdgcn_s_sleep(12);
          }
          MEMFENCE();
#pragma unroll
          for (int i = 0; i < 4; ++i) if (miss[i]) p[i] = ld128(rs, poff + i * (32 * D * 4));
        }
        f32x4 s = (__builtin_bit_cast(f32x4, p[0]) + __builtin_bit_cast(f32x4, p[1])) + (__builtin_bit_cast(f32x4, p[2]) + __builtin_bit_cast(f32x4, p[3]));
#pragma unroll
        for (int i = 0; i < 4; ++i) s[i] = xrow_sum(s[i]);
        return u32x2{pkbf(s[0], s[1]), pkbf(s[2], s[3])};
      };
      const u32x2 at = reduce16(HP_OFF, true);
      const u32x2 hv = {canonu(pkbf(bflo(xr[0]) + bflo(at[0]), bfhi(xr[0]) + bfhi(at[0]))), canonu(pkbf(bflo(xr[1]) + bflo(at[1]), bfhi(xr[1]) + bfhi(at[1])))};
      {
        const unsigned o0 = (unsigned)__builtin_amdgcn_mov_dpp((int)hv[0], XOR1, 0xf, 0xf, true), o1 = (unsigned)__builtin_amdgcn_mov_dpp((int)hv[1], XOR1, 0xf, 0xf, true);
        const bool odd = n & 1;
        st128(rs, H_OFF + (2 * a + (n & 1)) * H_REP + (t.rb * D + t.rc) * 2 + (n >> 1) * 16,
              u32x4{odd ? o0 : hv[0], odd ? o1 : hv[1], odd ? hv[0] : o0, odd ? hv[1] : o1});
      }
      MEMFENCE();
      const u32x2 y = reduce16(FP_OFF, false);
      if (a == 0) *(u32x2*)(out + t.rb * D + t.rc + n * 4) =
          u32x2{pkbf(bflo(hv[0]) + bflo(y[0]), bfhi(hv[0]) + bfhi(y[0])), pkbf(bflo(hv[1]) + bflo(y[1]), bfhi(hv[1]) + bfhi(y[1]))};
    }
    return;
  }
  if (!t.is_ffn) return;

  WG_BARRIER();
#pragma unroll 1
  for (int i = w; i < 72; i += 8) {
    const int row = i * 8 + (lane >> 3), c = (lane & 7) ^ (((row >> 1) & 1) | ((row >> 2) & 2));
    dma16((const char*)(c < 4 ? wg : wu) + (row * (FF * 2) + t.f * 64 + (c & 3) * 16), lbase + GU_L + i * 1024);
  }
#pragma unroll 1
  for (int i = w; i < 36; i += 8) {
    const int row = i * 8 + (lane >> 3), c = (lane & 7) ^ ((row & 2) | ((row >> 1) & 4));
    dma16nt((const char*)wd + ((t.kg * 288 + row) * 1152 + t.nsl * 128 + c * 16), lbase + DN_L + i * 1024);
  }
  if (w < 4) VMWAIT(5); else VMWAIT(4);
  WG_BARRIER();
  auto get_h = [&]() {
    const int hoff = H_OFF + xcd * H_REP + w * D * 2;
    if (w == 7) {
#pragma unroll 1
      for (unsigned spins = 0; spins <= SPIN_CAP; ++spins) {
        MEMFENCE();
        const unsigned gv = ld32(rs, GD_OFF + xcd * 128 + n * 4);
        if (!__any(gv == SENT)) break;
        __builtin_amdgcn_s_sleep(4);
      }
    }
    WG_BARRIER();
    u32x4 ha = ld128(rs, hoff + lane * 16);
    unsigned hb = ld32(rs, hoff + 1024 + (lane & 31) * 4);
#pragma unroll 1
    for (unsigned spins = 0; spins <= SPIN_CAP; ++spins) {
      const bool ma = mx4(ha) == SENT, mb = hb == SENT;
      if (!__any(ma || mb)) break;
      MEMFENCE();
      if (ma) ha = ld128(rs, hoff + lane * 16);
      if (mb) hb = ld32(rs, hoff + 1024 + (lane & 31) * 4);
    }
    VMWAIT(0);
    ln_row(ha, hb, lane, L + XN_L + w * XN_STR);
  };
  if (w < 4) {
    u32x4 AG[18];
    const int p = n & 3, q = n >> 2;
    const int aoff = GU_L + (8 * a + q) * 128 + (((p >> 1) * 4 + w) ^ ((q >> 1) | ((a & 1) << 1))) * 16 + (p & 1) * 8;
#pragma unroll
    for (int s = 0; s < 18; ++s) AG[s] = tr_frag(L, aoff + s * 4096, aoff + s * 4096 + 512);
    get_h();
    WG_BARRIER();
    u32x4 Bq[18];
#pragma unroll
    for (int s = 0; s < 18; ++s) Bq[s] = lds128(L, XN_L + (n & 7) * XN_STR + a * 16 + s * 64);
    f32x4 acc = {0.f, 0.f, 0.f, 0.f};
    PIN(acc);
#pragma unroll
    for (int s = 0; s < 18; ++s) acc = mfma16(AG[s], Bq[s], acc);
    const bool hi = lane >= 32, n8 = n >= 8;
    const float lo2 = n8 ? acc[1] : acc[0], hi2 = n8 ? acc[3] : acc[2];
    const float keep = hi ? hi2 : lo2, give = hi ? lo2 : hi2;
    auto sw = __builtin_amdgcn_permlane32_swap(__float_as_uint(give), __float_as_uint(give), false, false);
    const float got = __uint_as_float(hi ? sw[0] : sw[1]);
    const float g = rbf(hi ? got : keep), uu = rbf(hi ? keep : got);
    const float z = 0.7978845608028654f * (g + 0.044715f * g * g * g);
    const float ge = g * __builtin_amdgcn_rcpf(1.0f + __builtin_amdgcn_exp2f(-2.0f * 1.4426950408889634f * z));
    const float act = rbf(ge) * uu;
    const float nb = dpp<ROR8>(act);
    const unsigned d0 = canonu(pkbf(act, nb));
    auto s32 = __builtin_amdgcn_permlane32_swap(d0, d0, false, false);
    auto sa = __builtin_amdgcn_permlane16_swap(s32[0], s32[0], false, false);
    auto sb = __builtin_amdgcn_permlane16_swap(s32[1], s32[1], false, false);
    if (lane < 8) st128(rs, AC_OFF + t.f * 512 + w * 128 + lane * 16, u32x4{sa[0], sb[0], sa[1], sb[1]});
    MEMFENCE();
  } else if (w < 6) {
    get_h();
    WG_BARRIER();
    __builtin_amdgcn_s_sleep(4);
    u32x4 AD[18];
    {
      const int p = n & 3, q = n >> 2, sw = (q & 2) | ((a & 1) << 2);
      const int o0 = DN_L + (8 * a + q) * 128 + ((4 * (w - 4) + (p >> 1)) ^ sw) * 16 + (p & 1) * 8;
      const int o1 = DN_L + (8 * a + q) * 128 + ((4 * (w - 4) + 2 + (p >> 1)) ^ sw) * 16 + (p & 1) * 8;
#pragma unroll
      for (int s = 0; s < 9; ++s) { AD[2 * s] = tr_frag(L, o0 + s * 4096, o0 + s * 4096 + 512); AD[2 * s + 1] = tr_frag(L, o1 + s * 4096, o1 + s * 4096 + 512); }
    }
    const int boff = AC_OFF + t.kg * 9 * 512 + a * 128 + (n & 7) * 16;
    u32x4 Bf[9];
    __builtin_amdgcn_s_sleep(16);
#pragma unroll
    for (int s = 0; s < 9; ++s) Bf[s] = ld128(rs, boff + s * 512);
#pragma unroll 1
    for (unsigned spins = 0; spins <= SPIN_CAP; ++spins) {
      bool miss[9]; bool any = false;
#pragma unroll
      for (int s = 0; s < 9; ++s) { miss[s] = __any(mx4(Bf[s]) == SENT); any |= miss[s]; }
      if (!any) break;
      MEMFENCE();
#pragma unroll
      for (int s = 0; s < 9; ++s) if (miss[s]) Bf[s] = ld128(rs, boff + s * 512);
    }
    f32x4 c0 = {0.f, 0.f, 0.f, 0.f}, c1 = {0.f, 0.f, 0.f, 0.f};
#pragma unroll
    for (int s = 0; s < 9; ++s) { c0 = mfma16(AD[2 * s], Bf[s], c0); c1 = mfma16(AD[2 * s + 1], Bf[s], c1); }
    st128(rs, FP_OFF + ((t.kg * 8 + (n & 7)) * D + t.nsl * 64 + (w - 4) * 32 + (n >> 3) * 16 + 4 * a) * 4, canon4(n < 8 ? c0 : c1));
    MEMFENCE();
  } else {
    get_h();
    WG_BARRIER();
  }
}

extern "C" void kernel_launch(void* const* d_in, const int* in_sizes, int n_in,
                              void* d_out, int out_size, void* d_ws, size_t ws_size,
                              hipStream_t stream) {
  fused_block<<<NWG, NTHR, 0, stream>>>((const u16*)d_in[0], (const u16*)d_in[1], (const u16*)d_in[2], (const u16*)d_in[3],
                                        (const u16*)d_in[4], (const u16*)d_in[6], (const u16*)d_in[7], (const u16*)d_in[8],
                                        (u16*)d_out, (unsigned char*)d_ws);
}
```
